```python
import torch

def _softmax(x, axis=-1):
    return torch.softmax(x, dim=axis)
def _gelu(x):
    return torch.nn.functional.gelu(x, approximate="tanh")
def _silu(x):
    return torch.nn.functional.silu(x)
def _sigmoid(x):
    return torch.sigmoid(x)
def _layernorm(x, axis=-1, eps=1e-5):
    m = x.mean(dim=axis, keepdim=True)
    v = x.var(dim=axis, keepdim=True, unbiased=False)
    return (x - m) / torch.sqrt(v + eps)
def _scatter_add(tgt, idx, src, axis=0):
    return tgt.clone().index_add_(axis, idx, src)
def _segment_sum(src, seg, n):
    out = torch.zeros((n,) + src.shape[1:], dtype=src.dtype)
    return out.index_add_(0, seg, src)
def _pad(x, pw):
    flat = tuple(v for pair in reversed(pw) for v in pair)
    return torch.nn.functional.pad(x, flat)

kernel_name = 'standalone__deep_cross_pnn__recsys'

def setup_inputs():

    _w = (lambda a: (lambda t: t.to(torch.bfloat16) if t.is_floating_point() else t)(torch.as_tensor(a)))
    B, F, D, V = 16384, 100, 64, min(100000, 100000)
    H = 4 * D
    return {
        "idx": _w((lambda lo, hi, *s: torch.randint(lo, hi, s))(0, V, B, F).to(torch.int64)),
        "table": _w((lambda *s: torch.randn(*s, dtype=torch.bfloat16) * 1.6)(V, D) * (D ** -0.5)),
        "w_p": _w((lambda *s: torch.randn(*s, dtype=torch.bfloat16) * 0.05)(F * F) * ((F * F) ** -0.5)),
        "w1": _w((lambda *s: torch.randn(*s, dtype=torch.bfloat16) * 1.6)(F * D, H) * ((F * D) ** -0.5)),
        "b1": _w((lambda *s: torch.randn(*s, dtype=torch.bfloat16) * 12.8)(H) * 0.01),
        "w2": _w((lambda *s: torch.randn(*s, dtype=torch.bfloat16) * 0.05)(H) * (H ** -0.5)),
        "b2": _w((lambda *s: torch.randn(*s, dtype=torch.bfloat16) * 1.6)(1) * 0.01),
    }

def reference(idx, table, w_p, w1, b1, w2, b2):
    emb = table[idx]
    B = emb.shape[0]; F = emb.shape[1]; D = emb.shape[2]
    ip = torch.einsum('bid,bjd->bij', emb, emb)
    ip = ip.reshape(B, F * F)
    logit_p = ip @ w_p
    z = emb.reshape(B, F * D)
    h = _gelu(z @ w1 + b1)
    logit_d = h @ w2 + b2
    return logit_p + logit_d
```

```cpp
#include <hip/hip_runtime.h>
#include <hip/hip_bf16.h>
#include <stdint.h>
#include <type_traits>

typedef short bf16x8 __attribute__((ext_vector_type(8)));
typedef short s16x4 __attribute__((ext_vector_type(4)));
typedef float f32x4 __attribute__((ext_vector_type(4)));
typedef unsigned u32x4 __attribute__((ext_vector_type(4)));
typedef unsigned u32x2 __attribute__((ext_vector_type(2)));
typedef unsigned short u16;
#define LAS __attribute__((address_space(3)))
#define GAS __attribute__((address_space(1)))

constexpr int NB = 16384, NF = 100, NV = 100000;
constexpr int BR = 0;
constexpr int AR = 65536;
constexpr int PS = 98304;
constexpr int IA = 131072;
constexpr int IP = 156672;
constexpr int MB1 = 159872, MW2 = MB1 + 512, MLP = MW2 + 512, MRED = MLP + 1024, MCNT = MRED + 1024, MJUNK = MCNT + 16;
constexpr int LDS_BYTES = MJUNK + 256 + 240;
constexpr int WPS = 49152;
constexpr int XBYTES = 256 * 65536;
static_assert(LDS_BYTES <= 163840, "lds");

#define MFMA(a, b, c) __builtin_amdgcn_mfma_f32_16x16x32_bf16(a, b, c, 0, 0, 0)
#define SCHED() __builtin_amdgcn_sched_barrier(0)

__device__ __forceinline__ unsigned rfl(unsigned x) { return __builtin_amdgcn_readfirstlane(x); }
template <int OFF>
__device__ __forceinline__ void dma16(unsigned m0v, unsigned voff, __amdgpu_buffer_rsrc_t rs, unsigned soff) {
  asm volatile("s_mov_b32 m0, %0\n\ts_nop 0\n\tbuffer_load_dwordx4 %1, %2, %3 offen offset:%4 lds"
               :: "s"(m0v), "v"(voff), "s"(rs), "s"(soff), "n"(OFF) : "memory");
}
__device__ __forceinline__ void dma4(unsigned m0v, unsigned voff, __amdgpu_buffer_rsrc_t rs, unsigned soff) {
  asm volatile("s_mov_b32 m0, %0\n\ts_nop 0\n\tbuffer_load_dword %1, %2, %3 offen lds"
               :: "s"(m0v), "v"(voff), "s"(rs), "s"(soff) : "memory");
}
template <int N> __device__ __forceinline__ void wait_vm() { asm volatile("s_waitcnt vmcnt(%0)" :: "n"(N) : "memory"); }
__device__ __forceinline__ void bar() { asm volatile("s_waitcnt lgkmcnt(0)\n\ts_barrier" ::: "memory"); }
__device__ __forceinline__ bf16x8 lds128(unsigned a) { return *(const LAS bf16x8*)(size_t)a; }
__device__ __forceinline__ unsigned lds32(unsigned a) { return *(const LAS unsigned*)(size_t)a; }
__device__ __forceinline__ s16x4 ldstr(unsigned a) { return __builtin_amdgcn_ds_read_tr16_b64_v4i16((LAS s16x4*)(size_t)a); }
__device__ __forceinline__ bf16x8 ldstr2(unsigned a) {
  s16x4 lo = ldstr(a), hi = ldstr(a + 2048);
  return __builtin_shufflevector(lo, hi, 0, 1, 2, 3, 4, 5, 6, 7);
}
__device__ __forceinline__ float bf2f(u16 x) { return __uint_as_float((unsigned)x << 16); }
__device__ __forceinline__ float rbf(float x) { return __bfloat162float(__float2bfloat16(x)); }
__device__ __forceinline__ unsigned long long tagmix(unsigned long long x) { return x * 0x9E3779B97F4A7C15ull + 0x632BE59BD9B4E019ull; }

struct Deep {
  f32x4 acc[2][4][4];
  bf16x8 A[2][8];
  bf16x8 Bf[2][4];
  unsigned aaddr[2], baddr[4];
};

template <int Q, bool RD, bool BARRIER>
__device__ __forceinline__ void deep_phase(Deep& d) {
  constexpr int MH = Q & 1, P = Q & 1;
  constexpr unsigned AOFF = ((Q + 2) & 3) * 8192, BOFF = ((Q + 1) & 3) * 16384;
#pragma unroll
  for (int g = 0; g < 8; ++g) {
    const int ks = g >> 2, mt = g & 3;
    if (RD) {
      if (MH == 0) {
        if (g < 2) { d.Bf[1][2 * g] = ldstr2(d.baddr[2 * g] + BOFF); d.Bf[1][2 * g + 1] = ldstr2(d.baddr[2 * g + 1] + BOFF); }
        else if (g < 6) {
          const int i = 2 * (g - 2);
          d.A[P ^ 1][i] = lds128(d.aaddr[i >> 2] + AOFF + (i & 3) * 2048);
          d.A[P ^ 1][i + 1] = lds128(d.aaddr[(i + 1) >> 2] + AOFF + ((i + 1) & 3) * 2048);
        }
      } else {
        if (g < 4) {
          const int i = 2 * g;
          d.A[P ^ 1][i] = lds128(d.aaddr[i >> 2] + AOFF + (i & 3) * 2048);
          d.A[P ^ 1][i + 1] = lds128(d.aaddr[(i + 1) >> 2] + AOFF + ((i + 1) & 3) * 2048);
        } else if (g < 6) {
          const int n = 2 * (g - 4);
          d.Bf[0][n] = ldstr2(d.baddr[n] + BOFF); d.Bf[0][n + 1] = ldstr2(d.baddr[n + 1] + BOFF);
        }
      }
    }
#pragma unroll
    for (int nt = 0; nt < 4; ++nt) d.acc[MH][mt][nt] = MFMA(d.Bf[ks][nt], d.A[P][g], d.acc[MH][mt][nt]);
    SCHED();
  }
  if (BARRIER) { bar(); SCHED(); }
}

__device__ __forceinline__ constexpr int tile_id(int bi, int bj) { return bi * 7 - bi * (bi - 1) / 2 + (bj - bi); }

struct Pnn {
  f32x4 W[28];
  bf16x8 F[7][2];
  f32x4 accA, accB;
};
__device__ __forceinline__ void pnn_tile(Pnn& p, f32x4& acc, int bi, int bj) {
  f32x4 z = {0.f, 0.f, 0.f, 0.f};
  z = MFMA(p.F[bi][0], p.F[bj][0], z);
  z = MFMA(p.F[bi][1], p.F[bj][1], z);
  acc += z * p.W[tile_id(bi, bj)];
}
template <int CTRL> __device__ __forceinline__ float dpp_add(float x) {
  return x + __int_as_float(__builtin_amdgcn_update_dpp(0, __float_as_int(x), CTRL, 0xf, 0xf, false));
}

__global__ __launch_bounds__(512, 2) void pnn_deep_kernel(const int* __restrict__ idx, const u16* __restrict__ table, const u16* __restrict__ wp,
                                                          const u16* __restrict__ w1, const u16* __restrict__ b1, const u16* __restrict__ w2,
                                                          const u16* __restrict__ b2, u16* __restrict__ out, char* __restrict__ ws) {
  extern __shared__ __attribute__((aligned(16))) char lds[];
  const unsigned L = (unsigned)(size_t)lds;
  const int tid = threadIdx.x, lane = tid & 63;
  const int wv = rfl(tid >> 6), w = wv & 3;
  const int b = blockIdx.x, h = (b >> 3) & 1, s0 = ((b >> 4) * 8 + (b & 7)) * 128;
  const int c = lane & 15, g = lane >> 4;
  GAS unsigned long long* flags = (GAS unsigned long long*)(ws + XBYTES);
  const unsigned rdoff = (unsigned)(c * 128 + ((g ^ ((c >> 1) & 7)) * 16));

  if (wv < 4) {
    const unsigned long long xo = __hip_atomic_load(flags + 32 * b, __ATOMIC_RELAXED, __HIP_MEMORY_SCOPE_AGENT);
    const unsigned long long xi = __hip_atomic_load(flags + 32 * (b ^ 8), __ATOMIC_RELAXED, __HIP_MEMORY_SCOPE_AGENT);
    {
      const u32x2* g2 = (const u32x2*)idx;
      u32x2 v[13];
#pragma unroll
      for (int u = 0; u < 13; ++u) {
        int i = tid + 256 * u; i = i < 3200 ? i : 3199;
        const int sg = i / 25, cc = i - sg * 25;
        v[u] = g2[(size_t)(s0 + (sg ^ (64 - 64 * h))) * 50 + 25 * h + cc];
      }
      const u16 vb = b1[tid], vw = w2[tid];
#pragma unroll
      for (int u = 0; u < 13; ++u) { int i = tid + 256 * u; i = i < 3200 ? i : 3199; *(LAS u32x2*)(size_t)(L + IA + i * 8) = v[u]; }
      *(LAS u16*)(size_t)(L + MB1 + tid * 2) = vb;
      *(LAS u16*)(size_t)(L + MW2 + tid * 2) = vw;
      if (tid == 0) *(LAS unsigned*)(size_t)(L + MCNT) = 0u;
    }
    Deep d;
#pragma unroll
    for (int m = 0; m < 2; ++m)
#pragma unroll
      for (int i = 0; i < 4; ++i)
#pragma unroll
        for (int j = 0; j < 4; ++j) d.acc[m][i][j] = f32x4{0.f, 0.f, 0.f, 0.f};
    d.aaddr[0] = L + AR + rdoff; d.aaddr[1] = L + AR + (rdoff ^ 64);
    asm volatile("" : "+v"(d.aaddr[0]), "+v"(d.aaddr[1]));
    {
      const int qq = c >> 2, pp = c & 3, f = qq | ((g & 1) << 2);
#pragma unroll
      for (int nt = 0; nt < 4; ++nt) d.baddr[nt] = L + BR + (8 * g + qq) * 512 + (w >> 1) * 256 + (((4 * (w & 1) + nt) ^ f) * 32) + pp * 8;
    }
    bar();
    bar();
#pragma unroll
    for (int i = 0; i < 8; ++i) d.A[0][i] = lds128(d.aaddr[i >> 2] + 8192 + (i & 3) * 2048);
#pragma unroll
    for (int n = 0; n < 4; ++n) d.Bf[0][n] = ldstr2(d.baddr[n]);
    bar(); SCHED();
    for (int it = 0; it < 24; ++it) {
      deep_phase<0, true, true>(d); deep_phase<1, true, true>(d); deep_phase<2, true, true>(d); deep_phase<3, true, true>(d);
    }
    deep_phase<0, true, true>(d); deep_phase<1, true, true>(d); deep_phase<2, true, false>(d);
    const __amdgpu_buffer_rsrc_t rx = __builtin_amdgcn_make_buffer_rsrc((void*)ws, (short)0, XBYTES, 0x00020000);
#pragma unroll
    for (int i = 0; i < 4; ++i)
#pragma unroll
      for (int j = 0; j < 4; ++j)
        __builtin_amdgcn_raw_buffer_store_b128(__builtin_bit_cast(u32x4, d.acc[0][i][j]), rx, lane * 16 + (i * 4 + j) * 1024, b * 65536 + w * 16384, 16);
    SCHED();
    deep_phase<3, false, false>(d);
    wait_vm<0>();
    {
      unsigned old = 0;
      if (lane == 0) old = __hip_atomic_fetch_add((LAS unsigned*)(size_t)(L + MCNT), 1u, __ATOMIC_RELAXED, __HIP_MEMORY_SCOPE_WORKGROUP);
      if (lane == 0 && old == 3u) __hip_atomic_store(flags + 32 * b + 16, tagmix(xo), __ATOMIC_RELAXED, __HIP_MEMORY_SCOPE_AGENT);
    }
    {
      const unsigned long long want = tagmix(xi);
      for (unsigned spins = 0; spins < (1u << 20); ++spins) {
        const unsigned long long y = __hip_atomic_load(flags + 32 * (b ^ 8) + 16, __ATOMIC_RELAXED, __HIP_MEMORY_SCOPE_AGENT);
        if (rfl((unsigned)(y == want))) break;
        __builtin_amdgcn_s_sleep(2);
      }
      __builtin_amdgcn_fence(__ATOMIC_ACQUIRE, "wavefront");
      asm volatile("" ::: "memory");
    }
#pragma unroll
    for (int i = 0; i < 4; ++i)
#pragma unroll
      for (int j = 0; j < 4; ++j)
        d.acc[0][i][j] = __builtin_bit_cast(f32x4, __builtin_amdgcn_raw_buffer_load_b128(rx, lane * 16 + (i * 4 + j) * 1024, (b ^ 8) * 65536 + w * 16384, 16));
    float sum[4] = {0.f, 0.f, 0.f, 0.f};
#pragma unroll
    for (int j = 0; j < 4; ++j) {
      const s16x4 vb = *(const LAS s16x4*)(size_t)(L + MB1 + (w * 64 + j * 16 + g * 4) * 2);
      const s16x4 vw = *(const LAS s16x4*)(size_t)(L + MW2 + (w * 64 + j * 16 + g * 4) * 2);
#pragma unroll
      for (int r = 0; r < 4; ++r) {
        const float bb = bf2f((u16)vb[r]), ww = bf2f((u16)vw[r]);
#pragma unroll
        for (int i = 0; i < 4; ++i) {
          const float x = rbf(rbf(d.acc[1][i][j][r] + d.acc[0][i][j][r]) + bb);
          const float u = x * (0.7978845608028654f + 0.035677408136300125f * x * x);
          const float ge = x * __builtin_amdgcn_rcpf(1.f + __expf(-2.f * u));
          sum[i] += rbf(ge) * ww;
        }
      }
    }
#pragma unroll
    for (int i = 0; i < 4; ++i) {
      float s = sum[i];
      s += __shfl_xor(s, 16); s += __shfl_xor(s, 32);
      if (lane < 16) *(LAS float*)(size_t)(L + MRED + (w * 64 + i * 16 + lane) * 4) = s;
    }
    bar();
    if (wv == 0) {
      float ld = 0.f;
#pragma unroll
      for (int k = 0; k < 4; ++k) ld += *(const LAS float*)(size_t)(L + MRED + (k * 64 + lane) * 4);
      const f32x4 pv = *(const LAS f32x4*)(size_t)(L + MLP + lane * 16);
      const float lp = (pv[0] + pv[1]) + (pv[2] + pv[3]);
      const float o = rbf(lp) + rbf(rbf(ld) + bf2f(b2[0]));
      out[s0 + 64 * h + lane] = __builtin_bit_cast(u16, __float2bfloat16(o));
      if (lane == 0) __hip_atomic_store(flags + 32 * (b ^ 8), xi + 1ull, __ATOMIC_RELAXED, __HIP_MEMORY_SCOPE_AGENT);
    }
  } else {
    const __amdgpu_buffer_rsrc_t rt = __builtin_amdgcn_make_buffer_rsrc((void*)table, (short)0, NV * 128, 0x00020000);
    const __amdgpu_buffer_rsrc_t rw = __builtin_amdgcn_make_buffer_rsrc((void*)w1, (short)0, NF * 64 * 512, 0x00020000);
    const __amdgpu_buffer_rsrc_t ri = __builtin_amdgcn_make_buffer_rsrc((void*)idx, (short)0, NB * NF * 4, 0x00020000);
    const __amdgpu_buffer_rsrc_t rp = __builtin_amdgcn_make_buffer_rsrc((void*)wp, (short)0, NF * NF * 2, 0x00020000);
    const unsigned wf = (unsigned)((lane >> 5) | ((w & 1) << 2));
    const unsigned wsrc0 = (unsigned)(w * 4096 + (lane >> 5) * 512 + ((lane >> 4) & 1) * 256 + ((((lane >> 1) & 7) ^ wf) * 32) + (lane & 1) * 16);
    const unsigned wsrc1 = wsrc0 ^ 64u;
    const unsigned goff0 = (unsigned)(((lane & 7) ^ (lane >> 4)) * 16), goff1 = goff0 ^ 64u;
    const unsigned wbase = (unsigned)h * 50u * 32768u;
    const unsigned iaaddr = L + IA + (unsigned)((16 * w + (lane >> 3)) * 200);
    const unsigned ipaddr = L + IP + (unsigned)(w * 800 + (lane >> 3) * 4);
    const unsigned psb = L + PS + w * 8192;
    const unsigned pidx = (unsigned)(s0 + 64 * h + w) * 400u;
    const unsigned pfoff = (unsigned)(((b >> 4) * 8 + lane) * 128);

    auto issue_w = [&](unsigned gr, int part) {
      const unsigned m0v = L + BR + (gr & 3u) * 16384u + w * 4096, so = wbase + gr * 16384u;
      if (part == 0) { dma16<0>(m0v, wsrc0, rw, so); } else if (part == 1) { dma16<1024>(m0v, wsrc1, rw, so); }
      else if (part == 2) { dma16<2048>(m0v, wsrc0, rw, so); } else { dma16<3072>(m0v, wsrc1, rw, so); }
    };
    auto idx_a = [&](unsigned gr, int u) { return lds32(iaaddr + (gr & 1u) * 12800u + (gr >> 1) * 4u + u * 1600); };
    auto issue_a = [&](unsigned gr, int u, unsigned id) {
      dma16<0>(L + AR + ((gr + 1u) & 3u) * 8192u + w * 2048 + u * 1024, (id << 7) + (u ? goff1 : goff0), rt, 0u);
    };
    auto issue_p = [&](unsigned gr, int u, unsigned id) {
      dma16<0>(psb + (gr & 3u) * 2048u + u * 1024, (id << 7) + (u ? goff1 : goff0), rt, 0u);
    };
    auto issue_pidx = [&](unsigned j) {
      if (lane < 25) dma16<0>(L + IP + w * 800 + (j & 1u) * 400u, (unsigned)lane * 16u, ri, pidx + j * 1600u);
    };

    issue_pidx(0); issue_pidx(1);
#pragma unroll
    for (int u = 0; u < 5; ++u) dma16<0>(L + WPS + (w * 5 + u) * 1024, (unsigned)lane * 16u, rp, (unsigned)((w * 5 + u) * 1024));
#pragma unroll
    for (int gr = 0; gr < 3; ++gr)
#pragma unroll
      for (int p = 0; p < 4; ++p) issue_w(gr, p);
    if (lane < 8) dma4(L + MJUNK, pfoff, rw, wbase + (3u + w) * 16384u);
    wait_vm<13>();
    bar();
#pragma unroll
    for (int gr = 0; gr < 3; ++gr) {
      const unsigned a0 = idx_a(gr, 0), a1 = idx_a(gr, 1), p0 = lds32(ipaddr + gr * 64), p1 = lds32(ipaddr + gr * 64 + 32);
      issue_a(gr, 0, a0); issue_a(gr, 1, a1); issue_p(gr, 0, p0); issue_p(gr, 1, p1);
    }
    Pnn p;
    {
      const unsigned a_tr = L + WPS + (unsigned)((4 * g + (c >> 2)) * 200 + (c & 3) * 8);
      const unsigned a_rw = L + WPS + (unsigned)(c * 200 + g * 8);
#pragma unroll
      for (int bi = 0; bi < 7; ++bi)
#pragma unroll
        for (int bj = bi; bj < 7; ++bj) {
          const s16x4 x = ldstr(a_tr + bi * 3200 + bj * 32);
          f32x4 v;
#pragma unroll
          for (int r = 0; r < 4; ++r) v[r] = bf2f((u16)x[r]);
          if (bi != bj) {
            const s16x4 y = *(const LAS s16x4*)(size_t)(a_rw + bj * 3200 + bi * 32);
#pragma unroll
            for (int r = 0; r < 4; ++r) v[r] += bf2f((u16)y[r]);
          }
          if (bj == 6) {
            const bool ok = c < 4 && (bi < 6 || g == 0);
#pragma unroll
            for (int r = 0; r < 4; ++r) v[r] = ok ? v[r] : 0.f;
          }
          p.W[tile_id(bi, bj)] = v;
        }
#pragma unroll
      for (int i = 0; i < 7; ++i) { p.F[i][0] = bf16x8{0, 0, 0, 0, 0, 0, 0, 0}; p.F[i][1] = bf16x8{0, 0, 0, 0, 0, 0, 0, 0}; }
      p.accA = f32x4{0.f, 0.f, 0.f, 0.f}; p.accB = f32x4{0.f, 0.f, 0.f, 0.f};
    }
    wait_vm<4>();
    bar();

    auto slot = [&](auto JCc, auto Sc, unsigned j) {
      constexpr int JC = decltype(JCc)::value, s = decltype(Sc)::value;
      constexpr int SG = JC < 0 ? 0 : 7 * JC + s;
      constexpr bool dw = SG <= 96, dp = SG + 3 <= 111, last = JC == 16;
      constexpr int sp = s < 4 ? s + 3 : s - 4;
      const unsigned sg = 7u * j + s, gr = sg + 3u;
      const unsigned jp = s < 4 ? j : j + 1u;
      unsigned a0 = 0, a1 = 0, p0 = 0, p1 = 0;
      if (dw) { a0 = idx_a(gr, 0); a1 = idx_a(gr, 1); }
      if (dp) {
        p0 = lds32(ipaddr + (jp & 1u) * 400u + sp * 64);
        if (sp < 6) p1 = lds32(ipaddr + (jp & 1u) * 400u + sp * 64 + 32);
      }
      if (dw) issue_w(gr, 0);
      if (s < 3) {
#pragma unroll
        for (int bj = s + 4; bj < 7; ++bj) pnn_tile(p, p.accA, s, bj);
      }
      if (!last) {
        const unsigned fa = psb + (sg & 3u) * 2048u;
        p.F[s][0] = lds128(fa + rdoff); p.F[s][1] = lds128(fa + (rdoff ^ 64));
      }
      if (dw) { issue_a(gr, 0, a0); issue_w(gr, 1); }
      if (dp) {
        if (sp < 6) issue_p(gr, 0, p0);
        else { if (lane < 32) issue_p(gr, 0, p0); }
      }
      if (s < 3) asm volatile("" : "+v"(p.accA));
      if (s == 2) {
        float t = (p.accA[0] + p.accA[1]) + (p.accA[2] + p.accA[3]);
        t = dpp_add<0x128>(t); t = dpp_add<0x124>(t); t = dpp_add<0x122>(t); t = dpp_add<0x121>(t);
        if (c == 0) *(LAS float*)(size_t)(j > 0 ? L + MLP + ((4 * (j - 1) + w) * 4 + g) * 4 : L + MJUNK + 256 + g * 4) = t;
      }
      if (dw) { issue_w(gr, 2); issue_a(gr, 1, a1); issue_w(gr, 3); }
      if (dp) {
        if (sp < 6) issue_p(gr, 1, p1);
        else issue_pidx(JC >= 14 ? j : j + 2u);
      }
      if (JC < 0 || SG + 7 <= 99) { if ((sg & 3u) == (unsigned)w) { if (lane < 8) dma4(L + MJUNK, pfoff, rw, wbase + (gr + 4u) * 16384u); } }
      if (!last) {
        if (s == 0) p.accB = f32x4{0.f, 0.f, 0.f, 0.f};
#pragma unroll
        for (int dd = 3; dd >= 0; --dd) if (s - dd >= 0) pnn_tile(p, p.accB, s - dd, s);
        asm volatile("" : "+v"(p.accB));
        if (s == 6) p.accA = p.accB;
        if (SG <= 96) wait_vm<16>(); else if (SG <= 108) wait_vm<4>(); else wait_vm<0>();
        if (SG <= 98) bar();
      }
    };
#define IC(x) std::integral_constant<int, x>{}
#define ITER(JC, j) do { slot(IC(JC), IC(0), j); slot(IC(JC), IC(1), j); slot(IC(JC), IC(2), j); slot(IC(JC), IC(3), j); \
                         slot(IC(JC), IC(4), j); slot(IC(JC), IC(5), j); slot(IC(JC), IC(6), j); } while (0)
    for (unsigned j = 0; j < 13; ++j) ITER(-1, j);
    ITER(13, 13u); ITER(14, 14u); ITER(15, 15u);
    slot(IC(16), IC(0), 16u); slot(IC(16), IC(1), 16u); slot(IC(16), IC(2), 16u);
    bar();
  }
}

extern "C" void kernel_launch(void* const* d_in, const int* in_sizes, int n_in, void* d_out, int out_size, void* d_ws, size_t ws_size,
                              hipStream_t stream) {
  (void)in_sizes; (void)n_in; (void)out_size; (void)ws_size;
  (void)hipFuncSetAttribute((const void*)pnn_deep_kernel, hipFuncAttributeMaxDynamicSharedMemorySize, LDS_BYTES);
  pnn_deep_kernel<<<256, 512, LDS_BYTES, stream>>>((const int*)d_in[0], (const u16*)d_in[1], (const u16*)d_in[2], (const u16*)d_in[3],
                                                   (const u16*)d_in[4], (const u16*)d_in[5], (const u16*)d_in[6], (u16*)d_out, (char*)d_ws);
}
```

```cpp
#include <hip/hip_runtime.h>
#include <hip/hip_bf16.h>
#include <stdint.h>
#include <type_traits>

typedef short bf16x8 __attribute__((ext_vector_type(8)));
typedef short s16x4 __attribute__((ext_vector_type(4)));
typedef float f32x4 __attribute__((ext_vector_type(4)));
typedef unsigned u32x4 __attribute__((ext_vector_type(4)));
typedef unsigned short u16;
typedef unsigned long long u64;
typedef __amdgpu_buffer_rsrc_t rsrc_t;
#define LAS __attribute__((address_space(3)))
#define GAS __attribute__((address_space(1)))

constexpr int NB = 16384, NF = 100, NV = 100000;
constexpr int NS = 48;
constexpr int G1 = 2 * NS;
constexpr int GE = G1 + 2 * (NF - 2 * NS);
constexpr int BR = 0;
constexpr int GT = 114688;
constexpr int IA = 131072;
constexpr int IP = IA + 128 * 208;
constexpr int MLP = IP + 3200, MJUNK = MLP + 256;
constexpr int MRED = IA;
constexpr int LDS_BYTES = MJUNK + 256 + 16;
constexpr int WPS = 49152;
constexpr int XBYTES = 256 * 65536;
static_assert(LDS_BYTES <= 163840 && LDS_BYTES % 16 == 0, "lds");

#define MFMA(a, b, c) __builtin_amdgcn_mfma_f32_16x16x32_bf16(a, b, c, 0, 0, 0)
#define SCHED() __builtin_amdgcn_sched_barrier(0)
#define IC(x) std::integral_constant<int, (x)>{}

__device__ __forceinline__ unsigned rfl(unsigned x) { return __builtin_amdgcn_readfirstlane(x); }
template <int MI, int OFF>
__device__ __forceinline__ void dma16(unsigned mb, unsigned voff, u32x4 rs, unsigned soff) {
  asm volatile("s_add_u32 m0, %0, %4\n\ts_nop 0\n\tbuffer_load_dwordx4 %1, %2, %3 offen offset:%5 lds"
               :: "s"(mb), "v"(voff), "s"(rs), "s"(soff), "n"(MI), "n"(OFF) : "memory", "scc");
}
template <int MI>
__device__ __forceinline__ void gat16(unsigned mb, unsigned id, unsigned off, u32x4 rs) {
  unsigned t;
  asm volatile("s_add_u32 m0, %1, %5\n\tv_lshl_or_b32 %0, %2, 7, %3\n\tbuffer_load_dwordx4 %0, %4, 0 offen lds"
               : "=&v"(t) : "s"(mb), "v"(id), "v"(off), "s"(rs), "n"(MI) : "memory", "scc");
}
__device__ __forceinline__ void dma4(unsigned m0v, unsigned voff, u32x4 rs, unsigned soff) {
  asm volatile("s_mov_b32 m0, %0\n\ts_nop 0\n\tbuffer_load_dword %1, %2, %3 offen lds"
               :: "s"(m0v), "v"(voff), "s"(rs), "s"(soff) : "memory");
}
template <int N> __device__ __forceinline__ void wait_vm() { asm volatile("s_waitcnt vmcnt(%0)" :: "n"(N) : "memory"); }
__device__ __forceinline__ void wait_lds() { asm volatile("s_waitcnt lgkmcnt(0)" ::: "memory"); }
__device__ __forceinline__ void bar() { asm volatile("s_waitcnt lgkmcnt(0)\n\ts_barrier" ::: "memory"); }
__device__ __forceinline__ bf16x8 lds128(unsigned a) { return *(const LAS bf16x8*)(size_t)a; }
__device__ __forceinline__ unsigned lds32(unsigned a) { return *(const LAS unsigned*)(size_t)a; }
__device__ __forceinline__ s16x4 ldstr(unsigned a) { return __builtin_amdgcn_ds_read_tr16_b64_v4i16((LAS s16x4*)(size_t)a); }
__device__ __forceinline__ bf16x8 cat(s16x4 lo, s16x4 hi) { return __builtin_shufflevector(lo, hi, 0, 1, 2, 3, 4, 5, 6, 7); }
__device__ __forceinline__ float bf2f(u16 x) { return __uint_as_float((unsigned)x << 16); }
__device__ __forceinline__ u64 tagmix(u64 x) { return x * 0x9E3779B97F4A7C15ull + 0x632BE59BD9B4E019ull; }
__device__ __forceinline__ u64 ld_agent(GAS u64* p) { return __hip_atomic_load(p, __ATOMIC_RELAXED, __HIP_MEMORY_SCOPE_AGENT); }
__device__ __forceinline__ void st_agent(GAS u64* p, u64 v) { __hip_atomic_store(p, v, __ATOMIC_RELAXED, __HIP_MEMORY_SCOPE_AGENT); }
__device__ __forceinline__ u64 rfl64(u64 x) { return ((u64)rfl((unsigned)(x >> 32)) << 32) | rfl((unsigned)x); }

#define STAMP(k)
#define STAMPX(k)
#define STAMPOUT()

struct Deep {
  f32x4 acc[2][4][4];
  bf16x8 A[2][8];
  bf16x8 Bf[2][4];
  unsigned aaddr[2], baddr[4];
};
__device__ __forceinline__ void dmfma(f32x4& c, const bf16x8& a, const bf16x8& b) {
  asm volatile("v_mfma_f32_16x16x32_bf16 %0, %1, %2, %0" : "+v"(c) : "v"(a), "v"(b));
}
template <int Q, class H>
__device__ __forceinline__ void deep_phase(Deep& d, H hook) {
  constexpr int MH = Q & 1, P = Q & 1;
  constexpr unsigned AOFF = (3 - ((Q + 1) & 3)) * 16384, BOFF = ((Q + 1) & 3) * 16384;
  s16x4 blo[4];
#pragma unroll
  for (int m = 0; m < 32; ++m) {
    const int g = m >> 2, nt = m & 3, ks = g >> 2, mt = g & 3;
    dmfma(d.acc[MH][mt][nt], d.Bf[ks][nt], d.A[P][g]);
    const int rb = MH == 0 ? m : m - 16;
    const int ra = MH == 0 ? m - 8 : m;
    if (rb >= 0 && rb < 8) {
      const int n = rb >> 1;
      if (rb & 1) d.Bf[MH ^ 1][n] = cat(blo[n], ldstr(d.baddr[n] + BOFF + 2048));
      else blo[n] = ldstr(d.baddr[n] + BOFF);
    }
    if (ra >= 0 && ra < 16 && !(ra & 1)) { const int i = ra >> 1; d.A[P ^ 1][i] = lds128(d.aaddr[i >> 2] + AOFF + (i & 3) * 2048); }
    hook(m);
    SCHED();
  }
}
template <int Q, bool RD, class H>
__device__ __forceinline__ void own_phase(Deep& d, H hook) {
  constexpr int U = Q >> 1, KS = Q & 1;
  constexpr unsigned AOFF = (3 - ((Q + 1) & 3)) * 16384, BOFF = ((Q + 1) & 3) * 16384;
  s16x4 blo[4];
#pragma unroll
  for (int m = 0; m < 16; ++m) {
    const int mt = m >> 2, nt = m & 3;
    dmfma(d.acc[1][mt][nt], d.Bf[KS][nt], d.A[U][KS * 4 + mt]);
    if (RD && m < 8) {
      const int n = m >> 1;
      if (m & 1) d.Bf[KS ^ 1][n] = cat(blo[n], ldstr(d.baddr[n] + BOFF + 2048));
      else blo[n] = ldstr(d.baddr[n] + BOFF);
      if (KS == 1) d.A[U ^ 1][m] = lds128(d.aaddr[m >> 2] + AOFF + (m & 3) * 2048);
    }
    hook(m);
    SCHED();
  }
}

__device__ __forceinline__ constexpr int tile_id(int bi, int bj) { return bi * 7 - bi * (bi - 1) / 2 + (bj - bi); }
__device__ __forceinline__ constexpr bool t_prev(int s, int t) { return s < 3 && t > s; }
__device__ __forceinline__ constexpr int t_bi(int s, int t) { return t_prev(s, t) ? 3 + (t - s) : t; }
__device__ __forceinline__ constexpr int t_bj(int s, int t) { return t_prev(s, t) ? 6 - s : s; }
template <int CTRL, int RM> __device__ __forceinline__ float dpp_add(float x) {
  return x + __int_as_float(__builtin_amdgcn_update_dpp(0, __float_as_int(x), CTRL, RM, 0xf, false));
}
__device__ __forceinline__ float wave_sum63(float t) {
  t = dpp_add<0x128, 0xf>(t); t = dpp_add<0x124, 0xf>(t); t = dpp_add<0x122, 0xf>(t); t = dpp_add<0x121, 0xf>(t);
  t = dpp_add<0x142, 0xa>(t); t = dpp_add<0x143, 0xc>(t);
  return t;
}
__device__ __forceinline__ u32x4 mk_srd(const void* p, unsigned bytes) {
  const u64 a = (u64)p;
  return u32x4{(unsigned)a, (unsigned)(a >> 32) & 0xffffu, bytes, 0x00020000u};
}

__global__ __launch_bounds__(512, 2) void pnn_deep_kernel(const int* __restrict__ idx, const u16* __restrict__ table, const u16* __restrict__ wp,
                                                          const u16* __restrict__ w1, const u16* __restrict__ b1, const u16* __restrict__ w2,
                                                          const u16* __restrict__ b2, u16* __restrict__ out, char* __restrict__ ws) {
  extern __shared__ __attribute__((aligned(16))) char lds[];
  const unsigned L = (unsigned)(size_t)lds;
  const int tid = threadIdx.x, lane = tid & 63;
  const int wv = rfl(tid >> 6), w = wv & 3;
  const int b = blockIdx.x, h = (b >> 3) & 1, s0 = ((b >> 4) * 8 + (b & 7)) * 128;
  const int c = lane & 15, g = lane >> 4;
  u64 t0_ = 0, r0_ = 0; (void)t0_; (void)r0_; unsigned st_[8] = {}, sr_[8] = {}, sx_[10] = {}; (void)st_; (void)sr_; (void)sx_;
  STAMP(0);
  const u32x4 ri = mk_srd(idx, NB * NF * 4);
  if (lane < 52) {
    const unsigned r = (unsigned)lane / 13u, ch = (unsigned)lane - r * 13u;
    const unsigned rowm = (unsigned)((16 * wv) ^ (64 - 64 * h)), rowx = (unsigned)(16 * wv + 64 * h);
    const unsigned vo = ch < 12u ? (rowm + r) * 400u + (unsigned)h * (NS * 4u) + ch * 16u : (wv < 4 ? (rowx + r) * 400u + G1 * 4u : 0x80000000u);
    const unsigned so = (unsigned)s0 * 400u, mb = L + IA + wv * 3328;
    dma16<0, 0>(mb, vo, ri, so); dma16<832, 0>(mb, vo, ri, so + 1600u); dma16<1664, 0>(mb, vo, ri, so + 3200u); dma16<2496, 0>(mb, vo, ri, so + 4800u);
  }
  GAS u64* flags = (GAS u64*)(ws + XBYTES);
  const unsigned rdoff = (unsigned)(c * 128 + ((g ^ ((c >> 1) & 7)) * 16));

  u32x4 rw = mk_srd(w1, NF * 64 * 512);
  const unsigned wf = (unsigned)((lane >> 5) | ((w & 1) << 2));
  const unsigned wsrc0 = (unsigned)(w * 4096 + (lane >> 5) * 512 + ((lane >> 4) & 1) * 256 + ((((lane >> 1) & 7) ^ wf) * 32) + (lane & 1) * 16);
  const unsigned wsrc1 = wsrc0 ^ 64u;
  const unsigned mbW = L + BR + w * 4096;
  unsigned wso = (unsigned)h * (NS * 32768u);

  if (wv < 4) {
    auto prow = [&](unsigned o) {
      dma16<0, 0>(mbW + o, wsrc0, rw, wso + o); dma16<0, 1024>(mbW + o, wsrc1, rw, wso + o);
      dma16<0, 2048>(mbW + o, wsrc0, rw, wso + o); dma16<0, 3072>(mbW + o, wsrc1, rw, wso + o);
    };
    Deep d;
    s16x4 vb[4], vw[4];
#pragma unroll
    for (int j = 0; j < 4; ++j) { vb[j] = *(const s16x4*)(b1 + w * 64 + j * 16 + g * 4); vw[j] = *(const s16x4*)(w2 + w * 64 + j * 16 + g * 4); }
    const u64 xo = rfl64(ld_agent(flags + 32 * b)), xi = rfl64(ld_agent(flags + 32 * (b ^ 8)));
#pragma unroll
    for (int i = 0; i < 4; ++i)
#pragma unroll
      for (int j = 0; j < 4; ++j) {
        d.acc[0][i][j] = f32x4{0.f, 0.f, 0.f, 0.f};
#pragma unroll
        for (int r = 0; r < 4; ++r) d.acc[1][i][j][r] = bf2f((u16)vb[j][r]);
        asm volatile("" : "+v"(d.acc[0][i][j]), "+v"(d.acc[1][i][j]));
      }
    d.aaddr[0] = L + GT - 3 * 16384 + rdoff; d.aaddr[1] = L + GT - 3 * 16384 + (rdoff ^ 64);
    asm volatile("" : "+v"(d.aaddr[0]), "+v"(d.aaddr[1]));
    {
      const int qq = c >> 2, pp = c & 3, f = qq | ((g & 1) << 2);
#pragma unroll
      for (int nt = 0; nt < 4; ++nt) d.baddr[nt] = L + BR + (8 * g + qq) * 512 + (w >> 1) * 256 + (((4 * (w & 1) + nt) ^ f) * 32) + pp * 8;
    }
    STAMPX(7);
    wait_vm<0>();
    STAMPX(8);
    bar();
    STAMP(1);
    prow(0u); prow(16384u); prow(32768u);
    wait_vm<8>();
    bar();
    STAMP(2);
#pragma unroll
    for (int i = 0; i < 8; ++i) d.A[0][i] = lds128(d.aaddr[i >> 2] + 3 * 16384 + (i & 3) * 2048);
#pragma unroll
    for (int n = 0; n < 4; ++n) d.Bf[0][n] = cat(ldstr(d.baddr[n]), ldstr(d.baddr[n] + 2048));
    wait_vm<0>();
    bar(); SCHED();
    auto none = [](int) {};
    deep_phase<0>(d, none); bar(); SCHED(); deep_phase<1>(d, none); bar(); SCHED(); deep_phase<2>(d, none); bar(); SCHED();
#pragma unroll 1
    for (int it = 0; it < G1 / 4 - 1; ++it) {
      deep_phase<3>(d, none); bar(); SCHED(); deep_phase<0>(d, none); bar(); SCHED();
      deep_phase<1>(d, none); bar(); SCHED(); deep_phase<2>(d, none); bar(); SCHED();
    }
    STAMP(3);
    const rsrc_t rx = __builtin_amdgcn_make_buffer_rsrc((void*)ws, (short)0, XBYTES, 0x00020000);
    const unsigned xoff = lane * 16, xso = b * 32768 + w * 8192, xso_in = (b ^ 8) * 32768 + w * 8192;
    const u64 want = tagmix(xi);
    const GAS u64* yp = flags + 32 * (b ^ 8) + 16;
    u64 y;
    typedef __bf16 bf16x2_t __attribute__((ext_vector_type(2)));
    typedef float f32x2 __attribute__((ext_vector_type(2)));
    auto pk = [](float a, float b2_) { return __builtin_bit_cast(unsigned, __builtin_convertvector(f32x2{a, b2_}, bf16x2_t)); };
    auto send = [&](int t) {
      const f32x4 p = d.acc[0][t >> 1][2 * (t & 1)], q = d.acc[0][t >> 1][2 * (t & 1) + 1];
      __builtin_amdgcn_raw_buffer_store_b128(u32x4{pk(p[0], p[1]), pk(p[2], p[3]), pk(q[0], q[1]), pk(q[2], q[3])}, rx, xoff + (t & 3) * 1024, xso + (t >> 2) * 4096, 16);
    };
    u32x4 rcv[8];
    auto fetch = [&]() {
      asm volatile("" ::: "memory");
#pragma unroll
      for (int t = 0; t < 8; ++t) rcv[t] = __builtin_amdgcn_raw_buffer_load_b128(rx, xoff + (t & 3) * 1024, xso_in + (t >> 2) * 4096, 16);
    };
    deep_phase<3>(d, [&](int m) { if ((m & 3) == 1) send(m >> 2); }); bar(); SCHED();
    STAMPX(0);
    own_phase<0, true>(d, none); bar(); SCHED();
    own_phase<1, true>(d, none);
    STAMPX(1);
    wait_vm<0>(); bar(); SCHED();
    if (tid == 0) st_agent(flags + 32 * b + 16, tagmix(xo));
    STAMPX(2);
    own_phase<2, true>(d, none); bar(); SCHED();
    asm volatile("global_load_dwordx2 %0, %1, %2 sc1" : "=v"(y) : "v"(0u), "s"(yp) : "memory");
    own_phase<3, true>(d, none); bar(); SCHED();
    own_phase<0, true>(d, none); bar(); SCHED();
    STAMPX(3);
    asm volatile("s_waitcnt vmcnt(0)" : "+v"(y) :: "memory");
    bool got = rfl((unsigned)(y == want));
    if (got) fetch();
    asm volatile("global_load_dwordx2 %0, %1, %2 sc1" : "=v"(y) : "v"(0u), "s"(yp) : "memory");
    SCHED();
    own_phase<1, true>(d, none); bar(); SCHED();
    own_phase<2, true>(d, none); bar(); SCHED();
    sx_[5] = got;
    asm volatile("s_waitcnt vmcnt(0)" : "+v"(y) :: "memory");
    if (!got) { got = rfl((unsigned)(y == want)); if (got) fetch(); }
    SCHED();
    own_phase<3, false>(d, none); bar(); SCHED();
    __builtin_amdgcn_s_setprio(2);
    STAMPX(4);
    if (!got) {
      for (unsigned spins = 0; spins < (1u << 20); ++spins) {
        if (rfl((unsigned)(ld_agent((GAS u64*)yp) == want))) break;
        __builtin_amdgcn_s_sleep(1);
      }
      fetch();
    }
    sx_[6] = got;
    STAMP(5);
    {
      const unsigned one = g == (c >> 2) ? ((c & 1) ? 0x3F800000u : 0x00003F80u) : 0u;
      const unsigned lo = (c & 2) ? 0u : one, hi = (c & 2) ? one : 0u;
      bf16x8 sel[2] = {__builtin_bit_cast(bf16x8, u32x4{lo, hi, 0u, 0u}), __builtin_bit_cast(bf16x8, u32x4{0u, 0u, lo, hi})};
      asm volatile("s_nop 1" : "+v"(sel[0]), "+v"(sel[1]));
#pragma unroll
      for (int t = 0; t < 16; ++t) dmfma(d.acc[1][t >> 2][t & 3], sel[t & 1], __builtin_bit_cast(bf16x8, rcv[t >> 1]));
      asm volatile("s_nop 7\n\ts_nop 7" ::: "memory"); SCHED();
#pragma unroll
      for (int i = 0; i < 4; ++i) {
        float tot = 0.f;
#pragma unroll
        for (int j = 0; j < 4; ++j) {
#pragma unroll
          for (int r = 0; r < 4; ++r) {
            float x = d.acc[1][i][j][r];
            float x2 = x * x;
            asm("" : "+v"(x2));
            const float e = x * __builtin_fmaf(x2, -0.035677408136300125f * 2.f * 1.4426950408889634f, -0.7978845608028654f * 2.f * 1.4426950408889634f);
            const float rr = __builtin_amdgcn_rcpf(1.f + __builtin_amdgcn_exp2f(e));
            tot = __builtin_fmaf(x * rr, bf2f((u16)vw[j][r]), tot);
          }
        }
        *(LAS float*)(size_t)(L + MRED + ((i * 16 + c) * 17 + w * 4 + g) * 4) = tot;
      }
    }
    STAMP(6);
    bar();
    STAMP(7); STAMPOUT();
    if (wv == 0) {
      float ld = 0.f;
#pragma unroll
      for (int k = 0; k < 16; ++k) ld += *(const LAS float*)(size_t)(L + MRED + (lane * 17 + k) * 4);
      const float lp = *(const LAS float*)(size_t)(L + MLP + lane * 4);
      const float o = lp + (ld + bf2f(b2[0]));
      out[s0 + 64 * h + lane] = __builtin_bit_cast(u16, (__bf16)o);
      if (lane == 0) st_agent(flags + 32 * (b ^ 8), xi + 1ull);
    }
  } else {
    __builtin_amdgcn_s_setprio(1);
    u32x4 rt = mk_srd(table, NV * 128), ra = rt;
    const u32x4 rp = mk_srd(wp, NF * NF * 2);
    unsigned pidx = (unsigned)(s0 + 64 * h + w) * 400u;
    unsigned mbI = L + IP + w * 800;
    if (lane < 25) { dma16<0, 0>(mbI, (unsigned)lane * 16u, ri, pidx); dma16<400, 0>(mbI, (unsigned)lane * 16u, ri, pidx + 1600u); }
    pidx += 3200u;
    unsigned pso = wso + 7u * 16384u;
    const unsigned wjump = (unsigned)(1 - h) * (NS * 32768u);
    const unsigned rank = (unsigned)(b >> 4);
    const unsigned pfoff = lane < 8 ? (rank * 8 + lane) * 128u : 0x80000000u;
    const unsigned goff0 = (unsigned)(((lane & 7) ^ (lane >> 4)) * 16), goff1 = goff0 ^ 64u;
    const unsigned goffT = lane < 32 ? goff0 : 0x80000000u;
    unsigned iaaddr = L + IA + (unsigned)((16 * w + (lane >> 3)) * 208);
    unsigned ipaddr = L + IP + (unsigned)(w * 800 + (lane >> 3) * 4);
    int astep = 64 * 208, pstep = 400;
    const unsigned mbG = L + GT + w * 2048;
    const unsigned faddr0 = mbG + 8192 + rdoff, faddr1 = mbG + 8192 + (rdoff ^ 64);
    const unsigned taddr0 = c < 4 ? faddr0 : mbG + 8192 + 512 + (lane & 31) * 16, taddr1 = c < 4 ? faddr1 : taddr0;
    unsigned o16 = 0;

    unsigned jcur = 0;
    auto issue_w = [&](auto PARTc) {
      constexpr int PART = decltype(PARTc)::value;
      dma16<0, PART * 1024>(mbW + o16, (PART & 1) ? wsrc1 : wsrc0, rw, wso);
    };
    auto issue_a = [&](auto Uc, unsigned id) {
      constexpr int U = decltype(Uc)::value;
      gat16<U * 1024>(mbG - o16, id, U ? goff1 : goff0, ra);
    };
    auto issue_p = [&](auto SPc, auto Uc, unsigned id) {
      constexpr int SP = decltype(SPc)::value, U = decltype(Uc)::value;
      gat16<8192 + U * 1024>(mbG - o16, id, SP == 6 ? goffT : (U ? goff1 : goff0), rt);
    };
    unsigned na0, na1, np0, np1;
    auto fetch_idx = [&](auto SPc) {
      constexpr int SP = decltype(SPc)::value;
      na0 = lds32(iaaddr); na1 = lds32(iaaddr + 8 * 208);
      iaaddr += astep; astep = 4 - astep;
      np0 = lds32(ipaddr + SP * 64);
      if (SP < 6) np1 = lds32(ipaddr + SP * 64 + 32);
      if (SP == 6) { ipaddr += pstep; }
    };

    {
      const unsigned mb = L + WPS + w * 5120, so = w * 5120;
      dma16<0, 0>(mb, lane * 16u, rp, so); dma16<0, 1024>(mb, lane * 16u, rp, so); dma16<0, 2048>(mb, lane * 16u, rp, so);
      dma16<0, 3072>(mb, lane * 16u, rp, so); dma16<4096, 0>(mb, lane * 16u, rp, so + 4096u);
    }
    dma4(L + MJUNK, lane < 16 ? (unsigned)((2 * w + (lane >> 3)) * 16384) + (rank * 8 + (lane & 7)) * 128u : 0x80000000u, rw, wso);
    STAMPX(0);
    wait_vm<1>();
    STAMPX(1);
    bar();
    STAMPX(2);
    auto pro = [&](auto SPc) {
      fetch_idx(SPc);
      issue_a(IC(0), na0); issue_a(IC(1), na1);
      issue_p(SPc, IC(0), np0); issue_p(SPc, IC(1), np1);
      o16 += 16384u; wso += 16384u;
    };
    pro(IC(0)); pro(IC(1)); pro(IC(2));
    STAMPX(3);
    f32x4 W[28];
    bf16x8 F[7][2];
    float accA = 0.f, accB = 0.f;
    f32x4 z[4] = {};
    {
      const unsigned a_tr = L + WPS + (unsigned)((4 * g + (c >> 2)) * 200 + (c & 3) * 8);
      const unsigned a_rw = L + WPS + (unsigned)(c * 200 + g * 8);
#pragma unroll
      for (int bi = 0; bi < 7; ++bi)
#pragma unroll
        for (int bj = bi; bj < 7; ++bj) {
          const s16x4 x = ldstr(a_tr + bi * 3200 + bj * 32);
          f32x4 v;
#pragma unroll
          for (int r = 0; r < 4; ++r) v[r] = bf2f((u16)x[r]);
          if (bi != bj) {
            const s16x4 y = *(const LAS s16x4*)(size_t)(a_rw + bj * 3200 + bi * 32);
#pragma unroll
            for (int r = 0; r < 4; ++r) v[r] += bf2f((u16)y[r]);
          }
          if (bj == 6) {
            const bool ok = c < 4 && (bi < 6 || g == 0);
#pragma unroll
            for (int r = 0; r < 4; ++r) v[r] = ok ? v[r] : 0.f;
          }
          W[tile_id(bi, bj)] = v;
        }
#pragma unroll
      for (int i = 0; i < 7; ++i) { F[i][0] = bf16x8{0, 0, 0, 0, 0, 0, 0, 0}; F[i][1] = bf16x8{0, 0, 0, 0, 0, 0, 0, 0}; }
    }
    fetch_idx(IC(3));
    STAMPX(5);
    wait_vm<4>();
    STAMPX(6);
    bar();
    unsigned lpaddr = L + MJUNK + 256;

    auto slot = [&](auto Kc, auto TAILc, unsigned j) {
      constexpr int K = decltype(Kc)::value;
      constexpr bool TAIL = decltype(TAILc)::value == 1, LASTS = decltype(TAILc)::value == 2, onlyA = TAIL && K > 0;
      constexpr int SP = (K + 3) % 7, s = (K + 6) % 7;
      const unsigned a0 = na0, a1 = na1, p0 = np0, p1 = np1;
      if (!TAIL && !LASTS) {
        if (K == 2) wso += j == 13u ? wjump : 0u;
        if (K == 5) pso += j == 12u ? wjump : 0u;
        if (K == 3) { ra[2] = j == 14u ? 0u : ra[2]; rw[2] = j == 14u ? 0u : rw[2]; }
        issue_w(IC(0)); issue_a(IC(0), a0);
      }
      SCHED();
      constexpr int sz = (K + 5) % 7;
      constexpr bool zonlyA = TAIL && K > 1;
      if (sz == 0) { accA = accB; accB = 0.f; }
      auto ac2 = [&](int n) {
        const int t = n >> 1;
        if (!zonlyA || t_prev(sz, t)) {
          float& a = t_prev(sz, t) ? accA : accB;
#pragma unroll
          for (int r = 2 * (n & 1); r < 2 * (n & 1) + 2; ++r) a = __builtin_fmaf(z[t][r], W[tile_id(t_bi(sz, t), t_bj(sz, t))][r], a);
        }
      };
      f32x4 T[4];
#pragma unroll
      for (int n = 0; n < 8; ++n) {
        const int t = n & 3;
        ac2(n);
        asm volatile("" : "+v"(accA), "+v"(accB));
        if (!(TAIL && K == 4) && (!onlyA || t_prev(s, t))) {
          if (n < 4) T[t] = MFMA(F[t_bi(s, t)][0], F[t_bj(s, t)][0], (f32x4{0.f, 0.f, 0.f, 0.f}));
          else z[t] = MFMA(F[t_bi(s, t)][1], F[t_bj(s, t)][1], T[t]);
        }
        SCHED();
      }
      const unsigned o16n = (o16 + 16384u) & 0xC000u;
      if (LASTS) {
        if (K == 1) wait_vm<9>(); else if (K == 2) wait_vm<7>(); else if (K == 3) wait_vm<5>(); else if (K == 4) wait_vm<3>(); else if (K == 5) wait_vm<1>(); else if (K == 6) wait_vm<0>();
        constexpr unsigned FO = K < 3 ? ((105 + K) & 3) * 16384 : 8192 + (K - 3) * 16384;
        F[K][0] = lds128((K == 6 ? taddr0 : faddr0) - FO);
        F[K][1] = lds128((K == 6 ? taddr1 : faddr1) - FO);
      } else if (!TAIL) {
        F[K][0] = lds128((K == 6 ? taddr0 : faddr0) - o16n);
        F[K][1] = lds128((K == 6 ? taddr1 : faddr1) - o16n);
        fetch_idx(IC((SP + 1) % 7));
        issue_w(IC(1)); issue_p(IC(SP), IC(0), p0);
      }
      SCHED();
      if (sz == 2) {
        const float t = wave_sum63(accA);
        if (lane == 63) *(LAS float*)(size_t)lpaddr = t;
        lpaddr = L + MLP + (4u * j + w) * 4u;
      }
      SCHED();
      if (!TAIL && !LASTS) { issue_w(IC(2)); issue_a(IC(1), a1); }
      SCHED();
      if (!TAIL && !LASTS) {
        issue_w(IC(3));
        if (SP < 6) issue_p(IC(SP), IC(1), p1);
        else {
          if (lane < 25) dma16<0, 0>(mbI, (unsigned)lane * 16u, ri, pidx);
          pidx += 1600u; mbI += pstep; pstep = -pstep;
        }
        if ((o16 >> 14) == (unsigned)w) dma4(L + MJUNK, pfoff, rw, pso);
        o16 = o16n; wso += 16384u; pso += 16384u;
        wait_vm<16>();
        bar();
      }
      SCHED();
    };
#pragma unroll 1
    for (unsigned j = 0; j < 15; ++j) {
      jcur = j;
      slot(IC(0), IC(0), j); slot(IC(1), IC(0), j); slot(IC(2), IC(0), j); slot(IC(3), IC(0), j);
      slot(IC(4), IC(0), j); slot(IC(5), IC(0), j); slot(IC(6), IC(0), j);
    }
    STAMP(3);
    {
      unsigned q[7];
      q[0] = np0; q[1] = np1;
#pragma unroll
      for (int i = 2; i < 7; ++i) q[i] = lds32(ipaddr + 192 + i * 32);
      wait_vm<8>();
      gat16<0>(mbG, q[0], goff0, rt); gat16<1024>(mbG, q[1], goff1, rt);
      gat16<0>(mbG - 16384, q[2], goff0, rt); gat16<1024>(mbG - 16384, q[3], goff1, rt);
      gat16<0>(mbG - 32768, q[4], goff0, rt); gat16<1024>(mbG - 32768, q[5], goff1, rt);
      wait_vm<8>();
      gat16<0>(mbG - 49152, q[6], goffT, rt);
    }
    slot(IC(0), IC(2), 15u); slot(IC(1), IC(2), 15u); slot(IC(2), IC(2), 15u); slot(IC(3), IC(2), 15u);
    slot(IC(4), IC(2), 15u); slot(IC(5), IC(2), 15u); slot(IC(6), IC(2), 15u);
    slot(IC(0), IC(1), 16u);
    slot(IC(1), IC(1), 16u); slot(IC(2), IC(1), 16u); slot(IC(3), IC(1), 16u); slot(IC(4), IC(1), 16u);
    wait_vm<0>();
    STAMP(6);
    bar();
    STAMP(7); STAMPOUT();
  }
}

extern "C" void kernel_launch(void* const* d_in, const int* in_sizes, int n_in, void* d_out, int out_size, void* d_ws, size_t ws_size,
                              hipStream_t stream) {
  (void)in_sizes; (void)n_in; (void)out_size; (void)ws_size;
  (void)hipFuncSetAttribute((const void*)pnn_deep_kernel, hipFuncAttributeMaxDynamicSharedMemorySize, LDS_BYTES);
  pnn_deep_kernel<<<256, 512, LDS_BYTES, stream>>>((const int*)d_in[0], (const u16*)d_in[1], (const u16*)d_in[2], (const u16*)d_in[3],
                                                   (const u16*)d_in[4], (const u16*)d_in[5], (const u16*)d_in[6], (u16*)d_out, (char*)d_ws);
}
```

```cpp
#include <hip/hip_runtime.h>
#include <hip/hip_bf16.h>
#include <stdint.h>
#include <type_traits>

typedef short bf16x8 __attribute__((ext_vector_type(8)));
typedef short s16x4 __attribute__((ext_vector_type(4)));
typedef float f32x4 __attribute__((ext_vector_type(4)));
typedef float f32x2 __attribute__((ext_vector_type(2)));
typedef unsigned u32x4 __attribute__((ext_vector_type(4)));
typedef __bf16 bf16x2_t __attribute__((ext_vector_type(2)));
typedef unsigned short u16;
typedef unsigned long long u64;
#define LAS __attribute__((address_space(3)))
#define GAS __attribute__((address_space(1)))

constexpr int NB = 16384, NF = 100, NV = 100000;
constexpr int NS = 50;
constexpr int BR = 0;
constexpr int GT = 114688;
constexpr int IA = 131072;
constexpr int IP = IA + 128 * 208;
constexpr int MLP = IP + 3200, MJUNK = MLP + 256, MCNT = MJUNK + 256;
constexpr int MRED = IA;
constexpr int LDS_BYTES = MCNT + 16;
constexpr int WPS = 49152;
constexpr int XBYTES = 256 * 32768;
constexpr int CODE_LINES = 160;
static_assert(LDS_BYTES <= 163840 && LDS_BYTES % 16 == 0, "lds");

#define MFMA(a, b, c) __builtin_amdgcn_mfma_f32_16x16x32_bf16(a, b, c, 0, 0, 0)
#define SCHED() __builtin_amdgcn_sched_barrier(0)
#define IC(x) std::integral_constant<int, (x)>{}
#define ZERO4 (f32x4{0.f, 0.f, 0.f, 0.f})

__device__ __forceinline__ unsigned rfl(unsigned x) { return __builtin_amdgcn_readfirstlane(x); }
template <int MI, int OFF>
__device__ __forceinline__ void dma16(unsigned mb, unsigned voff, u32x4 rs, unsigned soff) {
  asm volatile("s_add_u32 m0, %0, %4\n\ts_nop 0\n\tbuffer_load_dwordx4 %1, %2, %3 offen offset:%5 lds"
               :: "s"(mb), "v"(voff), "s"(rs), "s"(soff), "n"(MI), "n"(OFF) : "memory", "scc");
}
template <int MI>
__device__ __forceinline__ void dma16m(unsigned mb, unsigned voff, u32x4 rs, unsigned soff) {
  asm volatile("s_add_u32 m0, %0, %4\n\ts_mov_b32 exec_hi, 0xfffff\n\tbuffer_load_dwordx4 %1, %2, %3 offen lds\n\ts_mov_b32 exec_hi, -1"
               :: "s"(mb), "v"(voff), "s"(rs), "s"(soff), "n"(MI) : "memory", "scc");
}
template <int MI>
__device__ __forceinline__ void gat16(unsigned mb, unsigned id, unsigned off, u32x4 rs) {
  unsigned t;
  asm volatile("s_add_u32 m0, %1, %5\n\tv_lshl_or_b32 %0, %2, 7, %3\n\tbuffer_load_dwordx4 %0, %4, 0 offen lds"
               : "=&v"(t) : "s"(mb), "v"(id), "v"(off), "s"(rs), "n"(MI) : "memory", "scc");
}
__device__ __forceinline__ void dma4(unsigned m0v, unsigned voff, u32x4 rs, unsigned soff) {
  asm volatile("s_mov_b32 m0, %0\n\ts_nop 0\n\tbuffer_load_dword %1, %2, %3 offen lds"
               :: "s"(m0v), "v"(voff), "s"(rs), "s"(soff) : "memory");
}
template <int N> __device__ __forceinline__ void wait_vm() { asm volatile("s_waitcnt vmcnt(%0)" :: "n"(N) : "memory"); }
__device__ __forceinline__ void wait_lds() { asm volatile("s_waitcnt lgkmcnt(0)" ::: "memory"); }
__device__ __forceinline__ void bar() { asm volatile("s_waitcnt lgkmcnt(0)\n\ts_barrier" ::: "memory"); }
__device__ __forceinline__ bf16x8 lds128(unsigned a) { return *(const LAS bf16x8*)(size_t)a; }
__device__ __forceinline__ unsigned lds32(unsigned a) { return *(const LAS unsigned*)(size_t)a; }
__device__ __forceinline__ s16x4 ldstr(unsigned a) { return __builtin_amdgcn_ds_read_tr16_b64_v4i16((LAS s16x4*)(size_t)a); }
__device__ __forceinline__ bf16x8 cat(s16x4 lo, s16x4 hi) { return __builtin_shufflevector(lo, hi, 0, 1, 2, 3, 4, 5, 6, 7); }
__device__ __forceinline__ float bf2f(u16 x) { return __uint_as_float((unsigned)x << 16); }
__device__ __forceinline__ u64 tagmix(u64 x) { return x * 0x9E3779B97F4A7C15ull + 0x632BE59BD9B4E019ull; }
__device__ __forceinline__ u64 ld_agent(GAS u64* p) { return __hip_atomic_load(p, __ATOMIC_RELAXED, __HIP_MEMORY_SCOPE_AGENT); }
__device__ __forceinline__ void st_agent(GAS u64* p, u64 v) { __hip_atomic_store(p, v, __ATOMIC_RELAXED, __HIP_MEMORY_SCOPE_AGENT); }
__device__ __forceinline__ u64 rfl64(u64 x) { return ((u64)rfl((unsigned)(x >> 32)) << 32) | rfl((unsigned)x); }
__device__ __forceinline__ u32x4 mk_srd(const void* p, unsigned bytes) {
  const u64 a = (u64)p;
  return u32x4{(unsigned)a, (unsigned)(a >> 32) & 0xffffu, bytes, 0x00020000u};
}

#define STAMP(k)
#define STAMPX(k)
#define STAMPOUT()

struct Deep {
  f32x4 acc[2][4][4];
  bf16x8 A[2][8];
  bf16x8 Bf[4][2];
  unsigned aaddr[2], baddr[2];
};
__device__ __forceinline__ void dmfma(f32x4& c, const bf16x8& a, const bf16x8& b) {
  asm volatile("v_mfma_f32_16x16x32_bf16 %0, %1, %2, %0" : "+v"(c) : "v"(a), "v"(b));
}
template <int Q, bool RD = true, class H>
__device__ __forceinline__ void deep_phase(Deep& d, H hook) {
  constexpr int MH = Q & 1, P = Q & 1;
  constexpr unsigned AOFF = (3 - ((Q + 1) & 3)) * 16384, BOFF = ((Q + 1) & 3) * 16384;
  s16x4 blo;
#pragma unroll
  for (int m = 0; m < 32; ++m) {
    const int nt = m >> 3, mt = ((m >> 1) & 3) ^ ((nt & 1) * 3), ks = m & 1;
    dmfma(d.acc[MH][mt][nt], d.Bf[nt][ks], d.A[P][ks * 4 + mt]);
    const int rb = MH == 0 ? m : (m >= 8 && m < 12 ? m - 8 : (m >= 16 && m < 20 ? m - 12 : -1));
    const int ra = MH == 0 ? m - 8 : (m < 8 ? m : (m >= 12 && m < 16 ? m - 4 : (m >= 20 ? m - 8 : -1)));
    if (RD && rb >= 0 && rb < 8) {
      const int u = rb >> 2, kk = (rb >> 1) & 1;
      if (rb & 1) d.Bf[2 * (MH ^ 1) + u][kk] = cat(blo, ldstr(d.baddr[u] + BOFF + kk * 8192 + 1024));
      else blo = ldstr(d.baddr[u] + BOFF + kk * 8192);
    }
    if (RD && ra >= 0 && ra < 16 && !(ra & 1)) { const int i = ra >> 1, ii = (i >> 1) + 4 * (i & 1); d.A[P ^ 1][ii] = lds128(d.aaddr[ii >> 2] + AOFF + (ii & 3) * 2048); }
    hook(m);
    SCHED();
  }
}

__device__ __forceinline__ constexpr int tile_id(int bi, int bj) { return bi * 7 - bi * (bi - 1) / 2 + (bj - bi); }
__device__ __forceinline__ constexpr bool t_prev(int s, int t) { return s < 3 && t > s; }
__device__ __forceinline__ constexpr int t_bi(int s, int t) { return t_prev(s, t) ? 3 + (t - s) : t; }
__device__ __forceinline__ constexpr int t_bj(int s, int t) { return t_prev(s, t) ? 6 - s : s; }

template <int CTRL, int RM> __device__ __forceinline__ float dpp_add(float x) {
  return x + __int_as_float(__builtin_amdgcn_update_dpp(0, __float_as_int(x), CTRL, RM, 0xf, false));
}
__device__ __forceinline__ float wave_sum63(float t) {
  t = dpp_add<0x128, 0xf>(t); t = dpp_add<0x124, 0xf>(t); t = dpp_add<0x122, 0xf>(t); t = dpp_add<0x121, 0xf>(t);
  t = dpp_add<0x142, 0xa>(t); t = dpp_add<0x143, 0xc>(t);
  return t;
}

__global__ __launch_bounds__(512, 2) void pnn_deep_kernel(const int* __restrict__ idx, const u16* __restrict__ table, const u16* __restrict__ wp,
                                                          const u16* __restrict__ w1, const u16* __restrict__ b1, const u16* __restrict__ w2,
                                                          const u16* __restrict__ b2, u16* __restrict__ out, char* __restrict__ ws) {
  extern __shared__ __attribute__((aligned(16))) char lds[];
  const unsigned L = (unsigned)(size_t)lds;
  const int tid = threadIdx.x, lane = tid & 63;
  const int wv = rfl(tid >> 6), w = wv & 3;
  const int b = blockIdx.x, h = (b >> 3) & 1, s0 = ((b >> 4) * 8 + (b & 7)) * 128;
  const int c = lane & 15, g = lane >> 4;
  STAMP(0);
  u64 pc0;
  asm volatile("s_getpc_b64 %0" : "=s"(pc0));
  const u32x4 rc = mk_srd((const void*)(pc0 & ~127ull), CODE_LINES * 128);
  const unsigned cvo = lane < 5 ? (unsigned)((b >> 3) * 5 + lane) * 128u : 0x80000000u;
  if (wv == 0) dma4(L + MJUNK, cvo, rc, 0u);

  const unsigned rdoff = (unsigned)(c * 128 + ((g ^ ((c >> 1) & 7)) * 16));
  const u32x4 rw = mk_srd(w1, NF * 64 * 512);
  const unsigned wsrc0 = (unsigned)(w * 8192 + (lane >> 4) * 512 + ((((lane >> 1) & 7) ^ (lane >> 4)) * 32) + (lane & 1) * 16);
  const unsigned wsrc1 = (wsrc0 ^ 128u) + 2048u;
  const unsigned mbW = L + BR + w * 4096;
  unsigned wso = (unsigned)h * (NS * 32768u);
  const unsigned r13 = (unsigned)lane / 13u, ch13 = (unsigned)lane - r13 * 13u;

  if (wv < 4) {
    GAS u64* flags = (GAS u64*)(ws + XBYTES);
    auto prow = [&](unsigned o, unsigned so) {
      dma16<0, 0>(mbW + o, wsrc0, rw, so); dma16<0, 1024>(mbW + o, wsrc0, rw, so + 1024u);
      dma16<0, 2048>(mbW + o, wsrc1, rw, so); dma16<0, 3072>(mbW + o, wsrc1, rw, so + 1024u);
    };
    Deep d;
    s16x4 vb[4];
    prow(0u, wso);
    {
      const u32x4 rp = mk_srd(wp, NF * NF * 2);
      const unsigned vo = r13 * 200u + ch13 * 16u, mb = L + WPS + w * (7 * 832), so = w * 5600;
      dma16m<0>(mb, vo, rp, so); dma16m<832>(mb, vo, rp, so + 800u); dma16m<1664>(mb, vo, rp, so + 1600u);
      dma16m<2496>(mb, vo, rp, so + 2400u); dma16m<3328>(mb, vo, rp, so + 3200u);
      dma16m<4160>(mb, vo, rp, so + 4000u); dma16m<4992>(mb, vo, rp, so + 4800u);
    }
    asm volatile("global_load_dwordx2 %0, %4, %5\n\tglobal_load_dwordx2 %1, %4, %5 offset:32\n\tglobal_load_dwordx2 %2, %4, %5 offset:256\n\t"
                 "global_load_dwordx2 %3, %4, %5 offset:288" : "=&v"(vb[0]), "=&v"(vb[1]), "=&v"(vb[2]), "=&v"(vb[3])
                 : "v"((unsigned)(w * 64 + g * 8)), "s"(b1) : "memory");
    STAMPX(7);
    wait_vm<4>();
    STAMPX(8);
    bar();
    prow(16384u, wso + 256u); prow(32768u, wso + 32768u);
    asm volatile("s_waitcnt vmcnt(8)" : "+v"(vb[0]), "+v"(vb[1]), "+v"(vb[2]), "+v"(vb[3]) :: "memory");
#pragma unroll
    for (int i = 0; i < 4; ++i)
#pragma unroll
      for (int j = 0; j < 4; ++j) {
        d.acc[0][i][j] = ZERO4;
#pragma unroll
        for (int r = 0; r < 4; ++r) d.acc[1][i][j][r] = bf2f((u16)vb[j][r]);
        asm volatile("" : "+v"(d.acc[0][i][j]), "+v"(d.acc[1][i][j]));
      }
    d.aaddr[0] = L + GT - 3 * 16384 + rdoff; d.aaddr[1] = L + GT - 3 * 16384 + (rdoff ^ 64);
    asm volatile("" : "+v"(d.aaddr[0]), "+v"(d.aaddr[1]));
    {
      const int qq = c >> 2, pp = c & 3, f = qq | ((g & 1) << 2);
#pragma unroll
      for (int u = 0; u < 2; ++u) d.baddr[u] = L + BR + (8 * g + qq) * 256 + (((2 * w + u) ^ f) * 32) + pp * 8;
    }
    if (tid == 0) *(LAS unsigned*)(size_t)(L + MCNT) = 0u;
    bar();
    STAMP(2);
#pragma unroll
    for (int i = 0; i < 8; ++i) d.A[0][i] = lds128(d.aaddr[i >> 2] + 3 * 16384 + (i & 3) * 2048);
#pragma unroll
    for (int n = 0; n < 4; ++n) d.Bf[n >> 1][n & 1] = cat(ldstr(d.baddr[n >> 1] + (n & 1) * 8192), ldstr(d.baddr[n >> 1] + (n & 1) * 8192 + 1024));
    wait_vm<4>();
    bar(); SCHED();
    auto none = [](int) {};
    deep_phase<0>(d, none);
    wait_vm<0>(); bar(); SCHED();
    u64 xov, xiv;
    unsigned b2v;
    asm volatile("global_load_ushort %0, %1, %2" : "=&v"(b2v) : "v"(0u), "s"(b2) : "memory");
    asm volatile("global_load_dwordx2 %0, %2, %3 sc1\n\tglobal_load_dwordx2 %1, %2, %4 sc1" : "=&v"(xov), "=&v"(xiv)
                 : "v"(0u), "s"(flags + 32 * b), "s"(flags + 32 * (b ^ 8)) : "memory");
    deep_phase<1>(d, none);
    bar(); SCHED();
    deep_phase<2>(d, none);
    asm volatile("s_waitcnt vmcnt(0)" : "+v"(xov), "+v"(xiv), "+v"(b2v) :: "memory");
    const u64 xo = rfl64(xov), xi = rfl64(xiv);
    const unsigned b2s = rfl(b2v);
    bar(); SCHED();
    STAMP(1);
#pragma unroll 1
    for (int it = 0; it < 24; ++it) {
      deep_phase<3>(d, none); bar(); SCHED(); deep_phase<0>(d, none); bar(); SCHED();
      deep_phase<1>(d, none); bar(); SCHED(); deep_phase<2>(d, none); bar(); SCHED();
    }
    __builtin_amdgcn_s_setprio(2);
    STAMP(3);
    const __amdgpu_buffer_rsrc_t rx = __builtin_amdgcn_make_buffer_rsrc((void*)ws, (short)0, XBYTES, 0x00020000);
    const unsigned xoff = lane * 16, xso = b * 32768 + w * 8192, xso_in = (b ^ 8) * 32768 + w * 8192;
    auto pk = [](float x, float y) { return __builtin_bit_cast(unsigned, __builtin_convertvector(f32x2{x, y}, bf16x2_t)); };
    deep_phase<3, false>(d, [&](int t) {
      if (t < 8) {
        const f32x4 p = d.acc[0][t >> 1][2 * (t & 1)], q = d.acc[0][t >> 1][2 * (t & 1) + 1];
        __builtin_amdgcn_raw_buffer_store_b128(u32x4{pk(p[0], p[1]), pk(p[2], p[3]), pk(q[0], q[1]), pk(q[2], q[3])}, rx,
                                               xoff + (t & 3) * 1024, xso + (t >> 2) * 4096, 16);
      }
    });
    STAMPX(0);
    wait_vm<0>();
    STAMPX(1);
    if (lane == 0) {
      const unsigned old = __hip_atomic_fetch_add((LAS unsigned*)(size_t)(L + MCNT), 1u, __ATOMIC_RELAXED, __HIP_MEMORY_SCOPE_WORKGROUP);
      if (old == 3u) st_agent(flags + 32 * b + 16, tagmix(xo));
    }
    s16x4 vw[4];
#pragma unroll
    for (int j = 0; j < 4; ++j) vw[j] = *(const s16x4*)(w2 + (j >> 1) * 128 + w * 32 + (j & 1) * 16 + g * 4);
    {
      const u64 want = tagmix(xi);
#pragma unroll 1
      for (unsigned spins = 0; spins < (1u << 20); ++spins) {
        if (rfl((unsigned)(ld_agent(flags + 32 * (b ^ 8) + 16) == want))) break;
        __builtin_amdgcn_s_sleep(1);
      }
    }
    STAMPX(2);
    asm volatile("" ::: "memory");
    u32x4 rcv[8];
#pragma unroll
    for (int t = 0; t < 8; ++t) { rcv[t] = __builtin_amdgcn_raw_buffer_load_b128(rx, xoff + (t & 3) * 1024, xso_in + (t >> 2) * 4096, 16); SCHED(); }
    STAMP(5);
    {
      const unsigned one = g == (c >> 2) ? ((c & 1) ? 0x3F800000u : 0x00003F80u) : 0u;
      const unsigned lo = (c & 2) ? 0u : one, hi = (c & 2) ? one : 0u;
      bf16x8 sel[2] = {__builtin_bit_cast(bf16x8, u32x4{lo, hi, 0u, 0u}), __builtin_bit_cast(bf16x8, u32x4{0u, 0u, lo, hi})};
      asm volatile("s_nop 1" : "+v"(sel[0]), "+v"(sel[1]));
#pragma unroll
      for (int i = 0; i < 4; ++i) {
#pragma unroll
        for (int j = 0; j < 4; ++j) dmfma(d.acc[1][i][j], sel[j & 1], __builtin_bit_cast(bf16x8, rcv[2 * i + (j >> 1)]));
        asm volatile("s_nop 7\n\ts_nop 3" : "+v"(d.acc[1][i][0]), "+v"(d.acc[1][i][1]), "+v"(d.acc[1][i][2]), "+v"(d.acc[1][i][3]));
        SCHED();
        if (i == 3) { if (tid == 0) st_agent(flags + 32 * (b ^ 8), xi + 1ull); }
        f32x2 e[8], sum = {0.f, 0.f};
#pragma unroll
        for (int k = 0; k < 8; ++k) {
          const f32x2 x = f32x2{d.acc[1][i][k >> 1][2 * (k & 1)], d.acc[1][i][k >> 1][2 * (k & 1) + 1]};
          e[k] = x * __builtin_elementwise_fma(x * x, (f32x2)(-0.035677408136300125f * 2.f * 1.4426950408889634f), (f32x2)(-0.7978845608028654f * 2.f * 1.4426950408889634f));
        }
        SCHED();
#pragma unroll
        for (int k = 0; k < 8; ++k) e[k] = f32x2{__builtin_amdgcn_exp2f(e[k][0]), __builtin_amdgcn_exp2f(e[k][1])};
        SCHED();
#pragma unroll
        for (int k = 0; k < 8; ++k) e[k] += (f32x2)(1.f);
        SCHED();
#pragma unroll
        for (int k = 0; k < 8; ++k) e[k] = f32x2{__builtin_amdgcn_rcpf(e[k][0]), __builtin_amdgcn_rcpf(e[k][1])};
        SCHED();
#pragma unroll
        for (int k = 0; k < 8; ++k) e[k] *= f32x2{d.acc[1][i][k >> 1][2 * (k & 1)], d.acc[1][i][k >> 1][2 * (k & 1) + 1]};
#pragma unroll
        for (int k = 0; k < 8; ++k) sum = __builtin_elementwise_fma(e[k], f32x2{bf2f((u16)vw[k >> 1][2 * (k & 1)]), bf2f((u16)vw[k >> 1][2 * (k & 1) + 1])}, sum);
        const float tot = sum[0] + sum[1];
        *(LAS float*)(size_t)(L + MRED + ((i * 16 + c) * 20 + w * 4 + g) * 4) = tot;
        SCHED();
      }
    }
    STAMP(6);
    bar();
    STAMP(7); STAMPOUT();
    if (wv == 0) {
      f32x4 v4 = *(const LAS f32x4*)(size_t)(L + MRED + lane * 80);
#pragma unroll
      for (int k = 1; k < 4; ++k) v4 += *(const LAS f32x4*)(size_t)(L + MRED + lane * 80 + k * 16);
      const float lp = *(const LAS float*)(size_t)(L + MLP + lane * 4);
      const float o = lp + (((v4[0] + v4[1]) + (v4[2] + v4[3])) + __uint_as_float(b2s << 16));
      out[s0 + 64 * h + lane] = __builtin_bit_cast(u16, (__bf16)o);
    }
  } else {
    __builtin_amdgcn_s_setprio(1);
    const u32x4 ri = mk_srd(idx, NB * NF * 4), rt = mk_srd(table, NV * 128);
    const unsigned iso0 = (unsigned)(s0 + 64 - 64 * h + 16 * w) * 400u + (unsigned)h * (NS * 4u), iso1 = (unsigned)(s0 + 64 * h + 16 * w) * 400u + (unsigned)h * (NS * 4u);
    u32x4 ia0[4];
    asm volatile("buffer_load_dwordx4 %0, %4, %5, %6 offen\n\tbuffer_load_dwordx4 %1, %4, %5, %6 offen offset:3200\n\t"
                 "buffer_load_dwordx4 %2, %4, %5, %7 offen\n\tbuffer_load_dwordx4 %3, %4, %5, %7 offen offset:3200"
                 : "=&v"(ia0[0]), "=&v"(ia0[1]), "=&v"(ia0[2]), "=&v"(ia0[3])
                 : "v"((unsigned)(lane >> 3) * 400u), "s"(ri), "s"(iso0), "s"(iso1) : "memory");
    unsigned pidx = (unsigned)(s0 + 64 * h + w) * 400u;
    unsigned mbI = L + IP + w * 800;
    if (lane < 25) dma16<0, 0>(mbI, (unsigned)lane * 16u, ri, pidx);
    const unsigned pfoff = lane < 8 ? (unsigned)((b >> 4) * 4 + (lane >> 1)) * 512u + (unsigned)(lane & 1) * 128u : 0x80000000u;
    const unsigned goff0 = (unsigned)(((lane & 7) ^ (lane >> 4)) * 16), goff1 = goff0 ^ 64u;
    const unsigned goffT = lane < 32 ? goff0 : 0x80000000u;
    unsigned iaaddr = L + IA + (unsigned)((16 * w + (lane >> 3)) * 208);
    const unsigned ipb = L + IP + (unsigned)(w * 800 + (lane >> 3) * 4);
    unsigned ipaddr = ipb;
    int astep = 64 * 208, pstep = 400;
    const unsigned mbG = L + GT + w * 2048;
    const unsigned faddr0 = mbG + 8192 + rdoff, faddr1 = mbG + 8192 + (rdoff ^ 64);
    const unsigned taddr0 = c < 4 ? faddr0 : mbG + 8192 + 512 + (lane & 31) * 16, taddr1 = c < 4 ? faddr1 : taddr0;
    unsigned o16 = 49152u;
    wso += 32768u + 256u;
    unsigned wstep = 32768u - 256u;
    STAMPX(0);
    asm volatile("s_waitcnt vmcnt(0)" : "+v"(ia0[0]), "+v"(ia0[1]), "+v"(ia0[2]), "+v"(ia0[3]) :: "memory");
    STAMPX(1);
    unsigned ip0[8];
#pragma unroll
    for (int q = 0; q < 8; ++q) ip0[q] = lds32(ipb + q * 32);
    gat16<0>(mbG, ia0[0][0], goff0, rt); gat16<1024>(mbG, ia0[1][0], goff1, rt);
    gat16<0>(mbG - 16384, ia0[2][0], goff0, rt); gat16<1024>(mbG - 16384, ia0[3][0], goff1, rt);
    gat16<8192>(mbG, ip0[0], goff0, rt); gat16<9216>(mbG, ip0[1], goff1, rt);
    gat16<8192>(mbG - 16384, ip0[2], goff0, rt); gat16<9216>(mbG - 16384, ip0[3], goff1, rt);
    if ((lane & 7) == 0) {
#pragma unroll
      for (int q = 0; q < 4; ++q) *(LAS u32x4*)(size_t)(iaaddr + (q >> 1) * (64 * 208) + (q & 1) * (8 * 208)) = ia0[q];
    }
    STAMPX(2);
    bar();
    STAMPX(3);
    f32x4 W[28];
    bf16x8 F[7][2];
    float accA = 0.f, accB = 0.f;
    f32x4 z[4];
    {
      const unsigned one = (c & 1) ? 0x3F800000u : 0x00003F80u;
      const int qd = (c & 7) >> 1;
      auto mksel = [&](bool on) { return __builtin_bit_cast(bf16x8, u32x4{on && qd == 0 ? one : 0u, on && qd == 1 ? one : 0u, on && qd == 2 ? one : 0u, on && qd == 3 ? one : 0u}); };
      const bf16x8 sel[3] = {mksel(g == (c >> 3)), mksel(g == 2 + (c >> 3)), mksel(g == 0 && c < 4)};
      const unsigned a = L + WPS + (unsigned)(c * 208 + g * 16), a3 = L + WPS + (unsigned)(c * 208 + 192);
      bf16x8 fr[7][4];
#pragma unroll
      for (int bk = 0; bk < 7; ++bk)
#pragma unroll
        for (int x = 0; x < 4; ++x) fr[bk][x] = lds128((x < 3 ? a + x * 64 : a3) + bk * 3328);
#pragma unroll
      for (int bi = 0; bi < 7; ++bi)
#pragma unroll
        for (int bj = bi; bj < 7; ++bj) {
          f32x4 v = MFMA(fr[bi][bj >> 1], sel[bj == 6 ? 2 : (bj & 1)], ZERO4);
          if (bi != bj) v = MFMA(sel[bi & 1], fr[bj][bi >> 1], v);
          W[tile_id(bi, bj)] = v;
        }
    }
    gat16<0>(mbG - 32768, ia0[0][1], goff0, rt); gat16<1024>(mbG - 32768, ia0[1][1], goff1, rt);
    gat16<8192>(mbG - 32768, ip0[4], goff0, rt); gat16<9216>(mbG - 32768, ip0[5], goff1, rt);
    if (lane < 25) dma16<400, 0>(mbI, (unsigned)lane * 16u, ri, pidx + 1600u);
    pidx += 3200u;
    dma4(L + MJUNK, pfoff, rw, wso - 256u + ((w + 1) >> 1) * 32768 + ((w + 1) & 1) * 256);
    unsigned na0 = ia0[2][1], na1 = ia0[3][1], np0 = ip0[6], np1 = ip0[7];
    iaaddr += 8;
    const unsigned ivo = r13 * 400u + ch13 * 16u, imb = L + IA + 16 * w * 208;
    STAMPX(5);
    wait_vm<6>();
    STAMPX(6);
    bar();
    unsigned lpaddr = L + MLP + w * 4;

    auto slot = [&](auto Kc, auto MODEc, unsigned j) {
      constexpr int K = decltype(Kc)::value;
      constexpr bool FIRST = decltype(MODEc)::value == 1, LAST = decltype(MODEc)::value == 2;
      constexpr int SP = (K + 3) % 7, s = (K + 6) % 7, sz = (K + 5) % 7;
      const unsigned a0 = na0, a1 = na1, p0 = np0, p1 = np1;
      unsigned q[6];
      if (LAST) {
#pragma unroll
        for (int i = 0; i < 6; ++i) q[i] = lds32(ipb + 192 + i * 32);
      }
      auto op = [&](int i) {
        if (i == 0) { if (!LAST) dma16<0, 0>(mbW + o16, wsrc0, rw, wso); else gat16<0>(mbW, q[0], goff0, rt); }
        if (i == 1) { if (!LAST) gat16<0>(mbG - o16, a0, goff0, rt); else gat16<1024>(mbW, q[1], goff1, rt); }
        if (i == 2) { if (!LAST) dma16<0, 1024>(mbW + o16, wsrc0, rw, wso + 1024u); else gat16<2048>(mbW, q[2], goff0, rt); }
        if (i == 3) { gat16<8192>(mbG - o16, p0, SP == 6 ? goffT : goff0, rt); }
        if (i == 4) { if (!LAST) dma16<0, 2048>(mbW + o16, wsrc1, rw, wso); else gat16<3072>(mbW, q[3], goff1, rt); }
        if (i == 5) { if (!LAST) gat16<1024>(mbG - o16, a1, goff1, rt); else gat16<0>(mbG, q[4], goff0, rt); }
        if (i == 6) { if (!LAST) dma16<0, 3072>(mbW + o16, wsrc1, rw, wso + 1024u); else gat16<1024>(mbG, q[5], goff1, rt); }
        if (i == 7) {
          if (SP < 6) gat16<9216>(mbG - o16, p1, goff1, rt);
          else {
            if (lane < 25) dma16<0, 0>(mbI, (unsigned)lane * 16u, ri, pidx);
            pidx += 1600u; mbI += pstep; pstep = -pstep;
          }
        }
      };
      op(0); op(1); op(2); op(3);
      SCHED();
      {
        if (sz == 0) { accA = accB; accB = 0.f; }
        auto ac2 = [&](int n) {
          const int tz = n >> 1;
          if (FIRST && (K < 2 || t_prev(sz, tz))) return;
          float& acc = t_prev(sz, tz) ? accA : accB;
#pragma unroll
          for (int r = 2 * (n & 1); r < 2 * (n & 1) + 2; ++r) acc = __builtin_fmaf(z[tz][r], W[tile_id(t_bi(sz, tz), t_bj(sz, tz))][r], acc);
        };
        ac2(0); ac2(1);
        SCHED();
#pragma unroll
        for (int n = 0; n < 8; ++n) {
          const int t = n >> 1;
          if (!(FIRST && (K == 0 || t_prev(s, t)))) {
            if (!(n & 1)) asm volatile("v_mfma_f32_16x16x32_bf16 %0, %1, %2, 0" : "=&v"(z[t]) : "v"(F[t_bi(s, t)][0]), "v"(F[t_bj(s, t)][0]));
            else dmfma(z[t], F[t_bi(s, t)][1], F[t_bj(s, t)][1]);
          }
          SCHED();
          if (n < 6) ac2(n + 2);
          SCHED();
        }
        asm volatile("" : "+v"(accA), "+v"(accB));
      }
      const unsigned o16n = (o16 + 16384u) & 0xC000u;
      F[K][0] = lds128((K == 6 ? taddr0 : faddr0) - o16n);
      F[K][1] = lds128((K == 6 ? taddr1 : faddr1) - o16n);
      if (!LAST) {
        constexpr int SN = (SP + 1) % 7;
        na0 = lds32(iaaddr); na1 = lds32(iaaddr + 8 * 208);
        iaaddr += astep; astep = 4 - astep;
        np0 = lds32(ipaddr + SN * 64);
        if (SN < 6) np1 = lds32(ipaddr + SN * 64 + 32);
        if (SN == 6) ipaddr += pstep;
      }
      asm volatile("s_nop 1");
      op(4); op(5);
      SCHED();
      if (sz == 2 && !FIRST) {
        const float t = wave_sum63(accA);
        if (lane == 63) *(LAS float*)(size_t)lpaddr = t;
        lpaddr += 16u;
      }
      SCHED();
      op(6); op(7);
      if (FIRST && K < 2) {
        const unsigned mb = imb + K * (64 * 208), so = K ? iso1 : iso0;
        dma16m<0>(mb, ivo, ri, so); dma16m<832>(mb, ivo, ri, so + 1600u); dma16m<1664>(mb, ivo, ri, so + 3200u); dma16m<2496>(mb, ivo, ri, so + 4800u);
      }
      if (!LAST) { if ((o16 >> 14) == (unsigned)w) dma4(L + MJUNK, pfoff, rw, wso + 65536u); }
      o16 = o16n; wso += wstep; wstep = 32768u - wstep;
      if (K == 5) { if (j == 12u && w == 0) dma4(L + MJUNK, cvo, rc, 0u); }
      wait_vm<((!FIRST || K >= 3) ? 16 : K == 1 ? 24 : 20)>();
      bar();
      SCHED();
    };
    slot(IC(0), IC(1), 0u); slot(IC(1), IC(1), 0u); slot(IC(2), IC(1), 0u); slot(IC(3), IC(1), 0u); slot(IC(4), IC(1), 0u); slot(IC(5), IC(1), 0u);
    STAMP(1);
#pragma unroll 1
    for (unsigned j = 1; j < 14; ++j) {
      slot(IC(6), IC(0), j - 1u);
      slot(IC(0), IC(0), j); slot(IC(1), IC(0), j); slot(IC(2), IC(0), j); slot(IC(3), IC(0), j); slot(IC(4), IC(0), j); slot(IC(5), IC(0), j);
    }
    slot(IC(6), IC(2), 13u);
    STAMP(3);
    {
      auto tile = [&](float& acc, int bi, int bj) {
        f32x4 v = MFMA(F[bi][0], F[bj][0], ZERO4);
        v = MFMA(F[bi][1], F[bj][1], v);
#pragma unroll
        for (int r = 0; r < 4; ++r) acc = __builtin_fmaf(v[r], W[tile_id(bi, bj)][r], acc);
      };
      unsigned q[14];
      q[13] = lds32(ipb + 384);
#pragma unroll
      for (int i = 0; i < 13; ++i) q[i] = lds32(ipb + 400 + i * 32);
      gat16<0>(mbG - 16384, q[13], goffT, rt); gat16<1024>(mbG - 16384, q[12], goffT, rt);
      gat16<16384>(mbW, q[0], goff0, rt); gat16<17408>(mbW, q[1], goff1, rt); gat16<18432>(mbW, q[2], goff0, rt); gat16<19456>(mbW, q[3], goff1, rt);
      gat16<8192>(mbG - 16384, q[4], goff0, rt); gat16<9216>(mbG - 16384, q[5], goff1, rt);
#pragma unroll
      for (int t = 0; t < 4; ++t)
#pragma unroll
        for (int r = 0; r < 4; ++r) accB = __builtin_fmaf(z[t][r], W[tile_id(t, 5)][r], accB);
#pragma unroll
      for (int t = 0; t < 4; ++t) tile(accB, t, 6);
      asm volatile("" : "+v"(accB));
      wait_vm<16>();
      bar(); SCHED();
      gat16<32768>(mbW, q[6], goff0, rt); gat16<33792>(mbW, q[7], goff1, rt); gat16<34816>(mbW, q[8], goff0, rt); gat16<35840>(mbW, q[9], goff1, rt);
      gat16<0>(mbG - 32768, q[10], goff0, rt); gat16<1024>(mbG - 32768, q[11], goff1, rt);
      bar(); SCHED();
      __builtin_amdgcn_s_setprio(0);
      tile(accB, 4, 4); tile(accB, 4, 5); tile(accB, 4, 6); tile(accB, 5, 5); tile(accB, 5, 6); tile(accB, 6, 6);
      const float t13 = wave_sum63(accB);
      if (lane == 63) *(LAS float*)(size_t)lpaddr = t13;
      const unsigned fw0 = mbW + rdoff, fw1 = mbW + (rdoff ^ 64);
      auto sample = [&](auto Sc) {
        constexpr int S = decltype(Sc)::value;
        if (S == 0) wait_vm<13>(); else wait_vm<0>();
#pragma unroll
        for (int k = 0; k < 7; ++k) {
          unsigned a0, a1;
          if (k == 6) { a0 = taddr0 - 8192 - 16384 + S * 1024; a1 = taddr1 - 8192 - 16384 + S * 1024; }
          else if (k == 5) { a0 = faddr0 - 8192 - S * 32768; a1 = faddr1 - 8192 - S * 32768; }
          else if (k >= 3) { a0 = fw0 + S * 32768 + (k - 3) * 2048; a1 = fw1 + S * 32768 + (k - 3) * 2048; }
          else if (S == 0) { a0 = faddr0 - ((98 + k) & 3) * 16384; a1 = faddr1 - ((98 + k) & 3) * 16384; }
          else if (k == 2) { a0 = faddr0 - 16384; a1 = faddr1 - 16384; }
          else { a0 = fw0 + 16384 + k * 2048; a1 = fw1 + 16384 + k * 2048; }
          F[k][0] = lds128(a0); F[k][1] = lds128(a1);
        }
        float acc = 0.f;
#pragma unroll
        for (int bj = 0; bj < 7; ++bj)
#pragma unroll
          for (int bi = 0; bi <= bj; ++bi) tile(acc, bi, bj);
        const float t = wave_sum63(acc);
        if (lane == 63) *(LAS float*)(size_t)(lpaddr + 16 + S * 16) = t;
      };
      sample(IC(0));
      STAMPX(8);
      sample(IC(1));
    }
    STAMP(6);
    bar();
    STAMP(7); STAMPOUT();
  }
}

extern "C" void kernel_launch(void* const* d_in, const int* in_sizes, int n_in, void* d_out, int out_size, void* d_ws, size_t ws_size,
                              hipStream_t stream) {
  (void)in_sizes; (void)n_in; (void)out_size; (void)ws_size;
  (void)hipFuncSetAttribute((const void*)pnn_deep_kernel, hipFuncAttributeMaxDynamicSharedMemorySize, LDS_BYTES);
  pnn_deep_kernel<<<256, 512, LDS_BYTES, stream>>>((const int*)d_in[0], (const u16*)d_in[1], (const u16*)d_in[2], (const u16*)d_in[3],
                                                   (const u16*)d_in[4], (const u16*)d_in[5], (const u16*)d_in[6], (u16*)d_out, (char*)d_ws);
}
```

```cpp
#include <hip/hip_runtime.h>
#include <hip/hip_bf16.h>
#include <stdint.h>
#include <type_traits>

typedef short bf16x8 __attribute__((ext_vector_type(8)));
typedef short s16x4 __attribute__((ext_vector_type(4)));
typedef float f32x4 __attribute__((ext_vector_type(4)));
typedef float f32x2 __attribute__((ext_vector_type(2)));
typedef unsigned u32x4 __attribute__((ext_vector_type(4)));
typedef __bf16 bf16x2_t __attribute__((ext_vector_type(2)));
typedef unsigned short u16;
typedef unsigned long long u64;
#define LAS __attribute__((address_space(3)))
#define GAS __attribute__((address_space(1)))

constexpr int NB = 16384, NF = 100, NV = 100000;
constexpr int NS = 50;
constexpr int BR = 0;
constexpr int GT = 114688;
constexpr int IA = 131072;
constexpr int IP = IA + 128 * 208;
constexpr int MLP = IP + 3200, MJUNK = MLP + 256, MCNT = MJUNK + 256;
constexpr int MRED = IA;
constexpr int LDS_BYTES = MCNT + 16;
constexpr int WPS = 49152;
constexpr int XBYTES = 256 * 32768;
static_assert(LDS_BYTES <= 163840 && LDS_BYTES % 16 == 0, "lds");

#define MFMA(a, b, c) __builtin_amdgcn_mfma_f32_16x16x32_bf16(a, b, c, 0, 0, 0)
#define SCHED() __builtin_amdgcn_sched_barrier(0)
#define IC(x) std::integral_constant<int, (x)>{}
#define ZERO4 (f32x4{0.f, 0.f, 0.f, 0.f})

__device__ __forceinline__ unsigned rfl(unsigned x) { return __builtin_amdgcn_readfirstlane(x); }
template <int MI, int OFF>
__device__ __forceinline__ void dma16(unsigned mb, unsigned voff, u32x4 rs, unsigned soff) {
  asm volatile("s_add_u32 m0, %0, %4\n\ts_nop 0\n\tbuffer_load_dwordx4 %1, %2, %3 offen offset:%5 lds"
               :: "s"(mb), "v"(voff), "s"(rs), "s"(soff), "n"(MI), "n"(OFF) : "memory", "scc");
}
template <int MI>
__device__ __forceinline__ void dma16m(unsigned mb, unsigned voff, u32x4 rs, unsigned soff) {
  asm volatile("s_add_u32 m0, %0, %4\n\ts_mov_b32 exec_hi, 0xfffff\n\tbuffer_load_dwordx4 %1, %2, %3 offen lds\n\ts_mov_b32 exec_hi, -1"
               :: "s"(mb), "v"(voff), "s"(rs), "s"(soff), "n"(MI) : "memory", "scc");
}
template <int MI>
__device__ __forceinline__ void gat16(unsigned mb, unsigned id, unsigned off, u32x4 rs) {
  unsigned t;
  asm volatile("s_add_u32 m0, %1, %5\n\tv_lshl_or_b32 %0, %2, 7, %3\n\tbuffer_load_dwordx4 %0, %4, 0 offen lds"
               : "=&v"(t) : "s"(mb), "v"(id), "v"(off), "s"(rs), "n"(MI) : "memory", "scc");
}
__device__ __forceinline__ void dma4(unsigned m0v, unsigned voff, u32x4 rs, unsigned soff) {
  asm volatile("s_mov_b32 m0, %0\n\ts_nop 0\n\tbuffer_load_dword %1, %2, %3 offen lds"
               :: "s"(m0v), "v"(voff), "s"(rs), "s"(soff) : "memory");
}
template <int N> __device__ __forceinline__ void wait_vm() { asm volatile("s_waitcnt vmcnt(%0)" :: "n"(N) : "memory"); }
__device__ __forceinline__ void bar() { asm volatile("s_waitcnt lgkmcnt(0)\n\ts_barrier" ::: "memory"); }
__device__ __forceinline__ bf16x8 lds128(unsigned a) { return *(const LAS bf16x8*)(size_t)a; }
__device__ __forceinline__ unsigned lds32(unsigned a) { return *(const LAS unsigned*)(size_t)a; }
__device__ __forceinline__ s16x4 ldstr(unsigned a) { return __builtin_amdgcn_ds_read_tr16_b64_v4i16((LAS s16x4*)(size_t)a); }
__device__ __forceinline__ bf16x8 cat(s16x4 lo, s16x4 hi) { return __builtin_shufflevector(lo, hi, 0, 1, 2, 3, 4, 5, 6, 7); }
__device__ __forceinline__ float bf2f(u16 x) { return __uint_as_float((unsigned)x << 16); }
__device__ __forceinline__ u64 tagmix(u64 x) { return x * 0x9E3779B97F4A7C15ull + 0x632BE59BD9B4E019ull; }
__device__ __forceinline__ u64 ld_agent(GAS u64* p) { return __hip_atomic_load(p, __ATOMIC_RELAXED, __HIP_MEMORY_SCOPE_AGENT); }
__device__ __forceinline__ void st_agent(GAS u64* p, u64 v) { __hip_atomic_store(p, v, __ATOMIC_RELAXED, __HIP_MEMORY_SCOPE_AGENT); }
__device__ __forceinline__ u64 rfl64(u64 x) { return ((u64)rfl((unsigned)(x >> 32)) << 32) | rfl((unsigned)x); }
__device__ __forceinline__ u32x4 mk_srd(const void* p, unsigned bytes) {
  const u64 a = (u64)p;
  return u32x4{(unsigned)a, (unsigned)(a >> 32) & 0xffffu, bytes, 0x00020000u};
}

struct Deep {
  f32x4 acc[2][4][4];
  bf16x8 A[2][8];
  bf16x8 Bf[4][2];
  unsigned aaddr[2], baddr[2];
};
__device__ __forceinline__ void dmfma(f32x4& c, const bf16x8& a, const bf16x8& b) {
  asm volatile("v_mfma_f32_16x16x32_bf16 %0, %1, %2, %0" : "+v"(c) : "v"(a), "v"(b));
}
template <int Q, bool RD = true, class H>
__device__ __forceinline__ void deep_phase(Deep& d, H hook) {
  constexpr int MH = Q & 1, P = Q & 1;
  constexpr unsigned AOFF = (3 - ((Q + 1) & 3)) * 16384, BOFF = ((Q + 1) & 3) * 16384;
  s16x4 blo;
#pragma unroll
  for (int m = 0; m < 32; ++m) {
    const int nt = m >> 3, mt = ((m >> 1) & 3) ^ ((nt & 1) * 3), ks = m & 1;
    dmfma(d.acc[MH][mt][nt], d.Bf[nt][ks], d.A[P][ks * 4 + mt]);
    const int rb = MH == 0 ? m : (m >= 8 && m < 12 ? m - 8 : (m >= 16 && m < 20 ? m - 12 : -1));
    const int ra = MH == 0 ? m - 8 : (m < 8 ? m : (m >= 12 && m < 16 ? m - 4 : (m >= 20 ? m - 8 : -1)));
    if (RD && rb >= 0 && rb < 8) {
      const int u = rb >> 2, kk = (rb >> 1) & 1;
      if (rb & 1) d.Bf[2 * (MH ^ 1) + u][kk] = cat(blo, ldstr(d.baddr[u] + BOFF + kk * 8192 + 1024));
      else blo = ldstr(d.baddr[u] + BOFF + kk * 8192);
    }
    if (RD && ra >= 0 && ra < 16 && !(ra & 1)) { const int i = ra >> 1, ii = (i >> 1) + 4 * (i & 1); d.A[P ^ 1][ii] = lds128(d.aaddr[ii >> 2] + AOFF + (ii & 3) * 2048); }
    hook(m);
    SCHED();
  }
}

__device__ __forceinline__ constexpr int tile_id(int bi, int bj) { return bi * 7 - bi * (bi - 1) / 2 + (bj - bi); }
__device__ __forceinline__ constexpr bool t_prev(int s, int t) { return s < 3 && t > s; }
__device__ __forceinline__ constexpr int t_bi(int s, int t) { return t_prev(s, t) ? 3 + (t - s) : t; }
__device__ __forceinline__ constexpr int t_bj(int s, int t) { return t_prev(s, t) ? 6 - s : s; }

template <int CTRL, int RM> __device__ __forceinline__ float dpp_add(float x) {
  return x + __int_as_float(__builtin_amdgcn_update_dpp(0, __float_as_int(x), CTRL, RM, 0xf, false));
}
__device__ __forceinline__ float wave_sum63(float t) {
  t = dpp_add<0x128, 0xf>(t); t = dpp_add<0x124, 0xf>(t); t = dpp_add<0x122, 0xf>(t); t = dpp_add<0x121, 0xf>(t);
  t = dpp_add<0x142, 0xa>(t); t = dpp_add<0x143, 0xc>(t);
  return t;
}

__global__ __launch_bounds__(512, 2) void pnn_deep_kernel(const int* __restrict__ idx, const u16* __restrict__ table, const u16* __restrict__ wp,
                                                          const u16* __restrict__ w1, const u16* __restrict__ b1, const u16* __restrict__ w2,
                                                          const u16* __restrict__ b2, u16* __restrict__ out, char* __restrict__ ws) {
  extern __shared__ __attribute__((aligned(16))) char lds[];
  const unsigned L = (unsigned)(size_t)lds;
  const int tid = threadIdx.x, lane = tid & 63;
  const int wv = rfl(tid >> 6), w = wv & 3;
  const int b = blockIdx.x, h = (b >> 3) & 1, s0 = ((b >> 4) * 8 + (b & 7)) * 128;
  const int c = lane & 15, g = lane >> 4;
  const unsigned rdoff = (unsigned)(c * 128 + ((g ^ ((c >> 1) & 7)) * 16));
  const u32x4 rw = mk_srd(w1, NF * 64 * 512);
  const unsigned wsrc0 = (unsigned)(w * 8192 + (lane >> 4) * 512 + ((((lane >> 1) & 7) ^ (lane >> 4)) * 32) + (lane & 1) * 16);
  const unsigned wsrc1 = (wsrc0 ^ 128u) + 2048u;
  const unsigned mbW = L + BR + w * 4096;
  unsigned wso = (unsigned)h * (NS * 32768u);
  const unsigned r13 = (unsigned)lane / 13u, ch13 = (unsigned)lane - r13 * 13u;

  if (wv < 4) {
    GAS u64* flags = (GAS u64*)(ws + XBYTES);
    auto prow = [&](unsigned o, unsigned so) {
      dma16<0, 0>(mbW + o, wsrc0, rw, so); dma16<0, 1024>(mbW + o, wsrc0, rw, so + 1024u);
      dma16<0, 2048>(mbW + o, wsrc1, rw, so); dma16<0, 3072>(mbW + o, wsrc1, rw, so + 1024u);
    };
    Deep d;
    s16x4 vb[4];
    prow(0u, wso);
    {
      const u32x4 rp = mk_srd(wp, NF * NF * 2);
      const unsigned vo = r13 * 200u + ch13 * 16u, mb = L + WPS + w * (7 * 832), so = w * 5600;
      dma16m<0>(mb, vo, rp, so); dma16m<832>(mb, vo, rp, so + 800u); dma16m<1664>(mb, vo, rp, so + 1600u);
      dma16m<2496>(mb, vo, rp, so + 2400u); dma16m<3328>(mb, vo, rp, so + 3200u);
      dma16m<4160>(mb, vo, rp, so + 4000u); dma16m<4992>(mb, vo, rp, so + 4800u);
    }
    asm volatile("global_load_dwordx2 %0, %4, %5\n\tglobal_load_dwordx2 %1, %4, %5 offset:32\n\tglobal_load_dwordx2 %2, %4, %5 offset:256\n\t"
                 "global_load_dwordx2 %3, %4, %5 offset:288" : "=&v"(vb[0]), "=&v"(vb[1]), "=&v"(vb[2]), "=&v"(vb[3])
                 : "v"((unsigned)(w * 64 + g * 8)), "s"(b1) : "memory");
    wait_vm<4>();
    bar();
    prow(16384u, wso + 256u); prow(32768u, wso + 32768u);
    asm volatile("s_waitcnt vmcnt(8)" : "+v"(vb[0]), "+v"(vb[1]), "+v"(vb[2]), "+v"(vb[3]) :: "memory");
#pragma unroll
    for (int i = 0; i < 4; ++i)
#pragma unroll
      for (int j = 0; j < 4; ++j) {
        d.acc[0][i][j] = ZERO4;
#pragma unroll
        for (int r = 0; r < 4; ++r) d.acc[1][i][j][r] = bf2f((u16)vb[j][r]);
        asm volatile("" : "+v"(d.acc[0][i][j]), "+v"(d.acc[1][i][j]));
      }
    d.aaddr[0] = L + GT - 3 * 16384 + rdoff; d.aaddr[1] = L + GT - 3 * 16384 + (rdoff ^ 64);
    asm volatile("" : "+v"(d.aaddr[0]), "+v"(d.aaddr[1]));
    {
      const int qq = c >> 2, pp = c & 3, f = qq | ((g & 1) << 2);
#pragma unroll
      for (int u = 0; u < 2; ++u) d.baddr[u] = L + BR + (8 * g + qq) * 256 + (((2 * w + u) ^ f) * 32) + pp * 8;
    }
    if (tid == 0) *(LAS unsigned*)(size_t)(L + MCNT) = 0u;
    bar();
#pragma unroll
    for (int i = 0; i < 8; ++i) d.A[0][i] = lds128(d.aaddr[i >> 2] + 3 * 16384 + (i & 3) * 2048);
#pragma unroll
    for (int n = 0; n < 4; ++n) d.Bf[n >> 1][n & 1] = cat(ldstr(d.baddr[n >> 1] + (n & 1) * 8192), ldstr(d.baddr[n >> 1] + (n & 1) * 8192 + 1024));
    wait_vm<4>();
    bar(); SCHED();
    auto none = [](int) {};
    u64 xov, xiv;
    unsigned b2v;
    asm volatile("global_load_ushort %0, %1, %2" : "=&v"(b2v) : "v"(0u), "s"(b2) : "memory");
    asm volatile("global_load_dwordx2 %0, %2, %3 sc1\n\tglobal_load_dwordx2 %1, %2, %4 sc1" : "=&v"(xov), "=&v"(xiv)
                 : "v"(0u), "s"(flags + 32 * b), "s"(flags + 32 * (b ^ 8)) : "memory");
    deep_phase<0>(d, none);
    wait_vm<3>(); bar(); SCHED();
    deep_phase<1>(d, none);
    asm volatile("s_waitcnt vmcnt(0)" : "+v"(xov), "+v"(xiv), "+v"(b2v) :: "memory");
    const u64 xo = rfl64(xov), xi = rfl64(xiv);
    const unsigned b2s = rfl(b2v);
    bar(); SCHED();
#pragma unroll 1
    for (int it = 0; it < 24; ++it) {
      deep_phase<2>(d, none); bar(); SCHED(); deep_phase<3>(d, none); bar(); SCHED();
      deep_phase<0>(d, none); bar(); SCHED(); deep_phase<1>(d, none); bar(); SCHED();
    }
    __builtin_amdgcn_s_setprio(2);
    const __amdgpu_buffer_rsrc_t rx = __builtin_amdgcn_make_buffer_rsrc((void*)ws, (short)0, XBYTES, 0x00020000);
    const unsigned xoff = lane * 16, xso = b * 32768 + w * 8192, xso_in = (b ^ 8) * 32768 + w * 8192;
    auto pk = [](float x, float y) { return __builtin_bit_cast(unsigned, __builtin_convertvector(f32x2{x, y}, bf16x2_t)); };
    auto send = [&](int t) {
      const f32x4 p = d.acc[0][t >> 1][2 * (t & 1)], q = d.acc[0][t >> 1][2 * (t & 1) + 1];
      __builtin_amdgcn_raw_buffer_store_b128(u32x4{pk(p[0], p[1]), pk(p[2], p[3]), pk(q[0], q[1]), pk(q[2], q[3])}, rx,
                                             xoff + (t & 3) * 1024, xso + (t >> 2) * 4096, 16);
    };
    deep_phase<2>(d, [&](int m) { if (m >= 19 && m <= 25 && (m & 1)) send(2 * (3 - ((m - 19) >> 1))); });
    deep_phase<3, false>(d, [&](int m) { if (m >= 3 && m <= 9 && (m & 1)) send(2 * (3 - ((m - 3) >> 1)) + 1); });
    wait_vm<0>();
    if (lane == 0) {
      const unsigned old = __hip_atomic_fetch_add((LAS unsigned*)(size_t)(L + MCNT), 1u, __ATOMIC_RELAXED, __HIP_MEMORY_SCOPE_WORKGROUP);
      if (old == 3u) st_agent(flags + 32 * b + 16, tagmix(xo));
    }
    s16x4 vw[4];
#pragma unroll
    for (int j = 0; j < 4; ++j) vw[j] = *(const s16x4*)(w2 + (j >> 1) * 128 + w * 32 + (j & 1) * 16 + g * 4);
    {
      const u64 want = tagmix(xi);
#pragma unroll 1
      for (unsigned spins = 0; spins < (1u << 20); ++spins) {
        if (rfl((unsigned)(ld_agent(flags + 32 * (b ^ 8) + 16) == want))) break;
        __builtin_amdgcn_s_sleep(1);
      }
    }
    asm volatile("" ::: "memory");
    u32x4 rcv[8];
#pragma unroll
    for (int t = 0; t < 8; ++t) { rcv[t] = __builtin_amdgcn_raw_buffer_load_b128(rx, xoff + (t & 3) * 1024, xso_in + (t >> 2) * 4096, 16); SCHED(); }
    {
      const unsigned one = g == (c >> 2) ? ((c & 1) ? 0x3F800000u : 0x00003F80u) : 0u;
      const unsigned lo = (c & 2) ? 0u : one, hi = (c & 2) ? one : 0u;
      bf16x8 sel[2] = {__builtin_bit_cast(bf16x8, u32x4{lo, hi, 0u, 0u}), __builtin_bit_cast(bf16x8, u32x4{0u, 0u, lo, hi})};
      asm volatile("s_nop 1" : "+v"(sel[0]), "+v"(sel[1]));
#pragma unroll
      for (int i = 0; i < 4; ++i) {
#pragma unroll
        for (int j = 0; j < 4; ++j) dmfma(d.acc[1][i][j], sel[j & 1], __builtin_bit_cast(bf16x8, rcv[2 * i + (j >> 1)]));
        asm volatile("s_nop 7\n\ts_nop 3" : "+v"(d.acc[1][i][0]), "+v"(d.acc[1][i][1]), "+v"(d.acc[1][i][2]), "+v"(d.acc[1][i][3]));
        SCHED();
        if (i == 3) { if (tid == 0) st_agent(flags + 32 * (b ^ 8), xi + 1ull); }
        f32x2 e[8], sum = {0.f, 0.f};
#pragma unroll
        for (int k = 0; k < 8; ++k) {
          const f32x2 x = f32x2{d.acc[1][i][k >> 1][2 * (k & 1)], d.acc[1][i][k >> 1][2 * (k & 1) + 1]};
          e[k] = x * __builtin_elementwise_fma(x * x, (f32x2)(-0.035677408136300125f * 2.f * 1.4426950408889634f), (f32x2)(-0.7978845608028654f * 2.f * 1.4426950408889634f));
        }
        SCHED();
#pragma unroll
        for (int k = 0; k < 8; ++k) e[k] = f32x2{__builtin_amdgcn_exp2f(e[k][0]), __builtin_amdgcn_exp2f(e[k][1])};
        SCHED();
#pragma unroll
        for (int k = 0; k < 8; ++k) e[k] += (f32x2)(1.f);
        SCHED();
#pragma unroll
        for (int k = 0; k < 8; ++k) e[k] = f32x2{__builtin_amdgcn_rcpf(e[k][0]), __builtin_amdgcn_rcpf(e[k][1])};
        SCHED();
#pragma unroll
        for (int k = 0; k < 8; ++k) e[k] *= f32x2{d.acc[1][i][k >> 1][2 * (k & 1)], d.acc[1][i][k >> 1][2 * (k & 1) + 1]};
#pragma unroll
        for (int k = 0; k < 8; ++k) sum = __builtin_elementwise_fma(e[k], f32x2{bf2f((u16)vw[k >> 1][2 * (k & 1)]), bf2f((u16)vw[k >> 1][2 * (k & 1) + 1])}, sum);
        const float tot = sum[0] + sum[1];
        *(LAS float*)(size_t)(L + MRED + ((i * 16 + c) * 20 + w * 4 + g) * 4) = tot;
        SCHED();
      }
    }
    bar();
    if (wv == 0) {
      f32x4 v4 = *(const LAS f32x4*)(size_t)(L + MRED + lane * 80);
#pragma unroll
      for (int k = 1; k < 4; ++k) v4 += *(const LAS f32x4*)(size_t)(L + MRED + lane * 80 + k * 16);
      const float lp = *(const LAS float*)(size_t)(L + MLP + lane * 4);
      const float o = lp + (((v4[0] + v4[1]) + (v4[2] + v4[3])) + __uint_as_float(b2s << 16));
      out[s0 + 64 * h + lane] = __builtin_bit_cast(u16, (__bf16)o);
    }
  } else {
    __builtin_amdgcn_s_setprio(1);
    const u32x4 ri = mk_srd(idx, NB * NF * 4), rt = mk_srd(table, NV * 128);
    const unsigned iso0 = (unsigned)(s0 + 64 - 64 * h + 16 * w) * 400u + (unsigned)h * (NS * 4u), iso1 = (unsigned)(s0 + 64 * h + 16 * w) * 400u + (unsigned)h * (NS * 4u);
    u32x4 ia0[4];
    asm volatile("buffer_load_dwordx4 %0, %4, %5, %6 offen\n\tbuffer_load_dwordx4 %1, %4, %5, %6 offen offset:3200\n\t"
                 "buffer_load_dwordx4 %2, %4, %5, %7 offen\n\tbuffer_load_dwordx4 %3, %4, %5, %7 offen offset:3200"
                 : "=&v"(ia0[0]), "=&v"(ia0[1]), "=&v"(ia0[2]), "=&v"(ia0[3])
                 : "v"((unsigned)(lane >> 3) * 400u), "s"(ri), "s"(iso0), "s"(iso1) : "memory");
    unsigned pidx = (unsigned)(s0 + 64 * h + w) * 400u;
    unsigned mbI = L + IP + w * 800;
    if (lane < 25) dma16<0, 0>(mbI, (unsigned)lane * 16u, ri, pidx);
    const unsigned pfoff = lane < 8 ? (unsigned)((b >> 4) * 4 + (lane >> 1)) * 512u + (unsigned)(lane & 1) * 128u : 0x80000000u;
    const unsigned goff0 = (unsigned)(((lane & 7) ^ (lane >> 4)) * 16), goff1 = goff0 ^ 64u;
    const unsigned goffT = lane < 32 ? goff0 : 0x80000000u;
    unsigned iaaddr = L + IA + (unsigned)((16 * w + (lane >> 3)) * 208);
    const unsigned ipb = L + IP + (unsigned)(w * 800 + (lane >> 3) * 4);
    unsigned ipaddr = ipb;
    int astep = 64 * 208, pstep = 400;
    const unsigned mbG = L + GT + w * 2048;
    const unsigned faddr0 = mbG + 8192 + rdoff, faddr1 = mbG + 8192 + (rdoff ^ 64);
    const unsigned taddr0 = c < 4 ? faddr0 : mbG + 8192 + 512 + (lane & 31) * 16, taddr1 = c < 4 ? faddr1 : taddr0;
    unsigned o16 = 49152u;
    wso += 32768u + 256u;
    unsigned wstep = 32768u - 256u;
    f32x4 W[28];
    bf16x8 F[7][2];
    float accA = 0.f, accB = 0.f;
    f32x4 z[4];
    unsigned ip0[8];
    asm volatile("s_waitcnt vmcnt(0)" : "+v"(ia0[0]), "+v"(ia0[1]), "+v"(ia0[2]), "+v"(ia0[3]) :: "memory");
#pragma unroll
    for (int q = 0; q < 8; ++q) ip0[q] = lds32(ipb + q * 32);
    gat16<0>(mbG, ia0[0][0], goff0, rt); gat16<1024>(mbG, ia0[1][0], goff1, rt);
    gat16<0>(mbG - 16384, ia0[2][0], goff0, rt); gat16<1024>(mbG - 16384, ia0[3][0], goff1, rt);
    gat16<8192>(mbG, ip0[0], goff0, rt); gat16<9216>(mbG, ip0[1], goff1, rt);
    gat16<8192>(mbG - 16384, ip0[2], goff0, rt); gat16<9216>(mbG - 16384, ip0[3], goff1, rt);
    if ((lane & 7) == 0) {
#pragma unroll
      for (int q = 0; q < 4; ++q) *(LAS u32x4*)(size_t)(iaaddr + (q >> 1) * (64 * 208) + (q & 1) * (8 * 208)) = ia0[q];
    }
    bar();
    {
      const unsigned one = (c & 1) ? 0x3F800000u : 0x00003F80u;
      const int qd = (c & 7) >> 1;
      auto mksel = [&](bool on) { return __builtin_bit_cast(bf16x8, u32x4{on && qd == 0 ? one : 0u, on && qd == 1 ? one : 0u, on && qd == 2 ? one : 0u, on && qd == 3 ? one : 0u}); };
      const bf16x8 sel[3] = {mksel(g == (c >> 3)), mksel(g == 2 + (c >> 3)), mksel(g == 0 && c < 4)};
      const unsigned a = L + WPS + (unsigned)(c * 208 + g * 16), a3 = L + WPS + (unsigned)(c * 208 + 192);
      bf16x8 fr[7][4];
#pragma unroll
      for (int bk = 0; bk < 7; ++bk)
#pragma unroll
        for (int x = 0; x < 4; ++x) fr[bk][x] = lds128((x < 3 ? a + x * 64 : a3) + bk * 3328);
#pragma unroll
      for (int bi = 0; bi < 7; ++bi)
#pragma unroll
        for (int bj = bi; bj < 7; ++bj) {
          f32x4 v = MFMA(fr[bi][bj >> 1], sel[bj == 6 ? 2 : (bj & 1)], ZERO4);
          if (bi != bj) v = MFMA(sel[bi & 1], fr[bj][bi >> 1], v);
          W[tile_id(bi, bj)] = v;
        }
    }
    gat16<0>(mbG - 32768, ia0[0][1], goff0, rt); gat16<1024>(mbG - 32768, ia0[1][1], goff1, rt);
    gat16<8192>(mbG - 32768, ip0[4], goff0, rt); gat16<9216>(mbG - 32768, ip0[5], goff1, rt);
    if (lane < 25) dma16<400, 0>(mbI, (unsigned)lane * 16u, ri, pidx + 1600u);
    pidx += 3200u;
    dma4(L + MJUNK, pfoff, rw, wso - 256u + ((w + 1) >> 1) * 32768 + ((w + 1) & 1) * 256);
    unsigned na0 = ia0[2][1], na1 = ia0[3][1], np0 = ip0[6], np1 = ip0[7];
    iaaddr += 8;
    const unsigned ivo = r13 * 400u + ch13 * 16u, imb = L + IA + 16 * w * 208;
    wait_vm<6>();
    bar();
    unsigned lpaddr = L + MLP + w * 4;

    auto slot = [&](auto Kc, auto MODEc) {
      constexpr int K = decltype(Kc)::value;
      constexpr bool FIRST = decltype(MODEc)::value == 1, LAST = decltype(MODEc)::value == 2;
      constexpr int SP = (K + 3) % 7, s = (K + 6) % 7, sz = (K + 5) % 7;
      const unsigned a0 = na0, a1 = na1, p0 = np0, p1 = np1;
      unsigned q[6];
      if (LAST) {
#pragma unroll
        for (int i = 0; i < 6; ++i) q[i] = lds32(ipb + 192 + i * 32);
      }
      auto op = [&](int i) {
        if (i == 0) { if (!LAST) dma16<0, 0>(mbW + o16, wsrc0, rw, wso); else gat16<0>(mbW, q[0], goff0, rt); }
        if (i == 1) { if (!LAST) gat16<0>(mbG - o16, a0, goff0, rt); else gat16<1024>(mbW, q[1], goff1, rt); }
        if (i == 2) { if (!LAST) dma16<0, 1024>(mbW + o16, wsrc0, rw, wso + 1024u); else gat16<2048>(mbW, q[2], goff0, rt); }
        if (i == 3) { gat16<8192>(mbG - o16, p0, SP == 6 ? goffT : goff0, rt); }
        if (i == 4) { if (!LAST) dma16<0, 2048>(mbW + o16, wsrc1, rw, wso); else gat16<3072>(mbW, q[3], goff1, rt); }
        if (i == 5) { if (!LAST) gat16<1024>(mbG - o16, a1, goff1, rt); else gat16<0>(mbG, q[4], goff0, rt); }
        if (i == 6) { if (!LAST) dma16<0, 3072>(mbW + o16, wsrc1, rw, wso + 1024u); else gat16<1024>(mbG, q[5], goff1, rt); }
        if (i == 7) {
          if (SP < 6) gat16<9216>(mbG - o16, p1, goff1, rt);
          else {
            if (lane < 25) dma16<0, 0>(mbI, (unsigned)lane * 16u, ri, pidx);
            pidx += 1600u; mbI += pstep; pstep = -pstep;
          }
        }
      };
      op(0); op(1); op(2); op(3);
      SCHED();
      {
        if (sz == 0) { accA = accB; accB = 0.f; }
        auto ac2 = [&](int n) {
          const int tz = n >> 1;
          if (FIRST && (K < 2 || t_prev(sz, tz))) return;
          float& acc = t_prev(sz, tz) ? accA : accB;
#pragma unroll
          for (int r = 2 * (n & 1); r < 2 * (n & 1) + 2; ++r) acc = __builtin_fmaf(z[tz][r], W[tile_id(t_bi(sz, tz), t_bj(sz, tz))][r], acc);
        };
        ac2(0); ac2(1);
        SCHED();
#pragma unroll
        for (int n = 0; n < 8; ++n) {
          const int t = n >> 1;
          if (!(FIRST && (K == 0 || t_prev(s, t)))) {
            if (!(n & 1)) asm volatile("v_mfma_f32_16x16x32_bf16 %0, %1, %2, 0" : "=&v"(z[t]) : "v"(F[t_bi(s, t)][0]), "v"(F[t_bj(s, t)][0]));
            else dmfma(z[t], F[t_bi(s, t)][1], F[t_bj(s, t)][1]);
          }
          SCHED();
          if (n < 6) ac2(n + 2);
          SCHED();
        }
        asm volatile("" : "+v"(accA), "+v"(accB));
      }
      const unsigned o16n = (o16 + 16384u) & 0xC000u;
      F[K][0] = lds128((K == 6 ? taddr0 : faddr0) - o16n);
      F[K][1] = lds128((K == 6 ? taddr1 : faddr1) - o16n);
      if (!LAST) {
        constexpr int SN = (SP + 1) % 7;
        na0 = lds32(iaaddr); na1 = lds32(iaaddr + 8 * 208);
        iaaddr += astep; astep = 4 - astep;
        np0 = lds32(ipaddr + SN * 64);
        if (SN < 6) np1 = lds32(ipaddr + SN * 64 + 32);
        if (SN == 6) ipaddr += pstep;
      }
      asm volatile("s_nop 1");
      op(4); op(5);
      SCHED();
      if (sz == 2 && !FIRST) {
        const float t = wave_sum63(accA);
        if (lane == 63) *(LAS float*)(size_t)lpaddr = t;
        lpaddr += 16u;
      }
      SCHED();
      op(6); op(7);
      if (FIRST && K < 2) {
        const unsigned mb = imb + K * (64 * 208), so = K ? iso1 : iso0;
        dma16m<0>(mb, ivo, ri, so); dma16m<832>(mb, ivo, ri, so + 1600u); dma16m<1664>(mb, ivo, ri, so + 3200u); dma16m<2496>(mb, ivo, ri, so + 4800u);
      }
      if (!LAST) { if ((o16 >> 14) == (unsigned)w) dma4(L + MJUNK, pfoff, rw, wso + 65536u); }
      o16 = o16n; wso += wstep; wstep = 32768u - wstep;
      wait_vm<((!FIRST || K >= 3) ? 16 : K == 1 ? 24 : 20)>();
      bar();
      SCHED();
    };
    slot(IC(0), IC(1)); slot(IC(1), IC(1)); slot(IC(2), IC(1)); slot(IC(3), IC(1)); slot(IC(4), IC(1)); slot(IC(5), IC(1));
#pragma unroll 1
    for (int j = 1; j < 14; ++j) {
      slot(IC(6), IC(0));
      slot(IC(0), IC(0)); slot(IC(1), IC(0)); slot(IC(2), IC(0)); slot(IC(3), IC(0)); slot(IC(4), IC(0)); slot(IC(5), IC(0));
    }
    slot(IC(6), IC(2));
    {
      auto tile = [&](float& acc, int bi, int bj) {
        f32x4 v = MFMA(F[bi][0], F[bj][0], ZERO4);
        v = MFMA(F[bi][1], F[bj][1], v);
#pragma unroll
        for (int r = 0; r < 4; ++r) acc = __builtin_fmaf(v[r], W[tile_id(bi, bj)][r], acc);
      };
      unsigned q[14];
      q[13] = lds32(ipb + 384);
#pragma unroll
      for (int i = 0; i < 13; ++i) q[i] = lds32(ipb + 400 + i * 32);
      gat16<0>(mbG - 16384, q[13], goffT, rt); gat16<1024>(mbG - 16384, q[12], goffT, rt);
      gat16<16384>(mbW, q[0], goff0, rt); gat16<17408>(mbW, q[1], goff1, rt); gat16<18432>(mbW, q[2], goff0, rt); gat16<19456>(mbW, q[3], goff1, rt);
      gat16<8192>(mbG - 16384, q[4], goff0, rt); gat16<9216>(mbG - 16384, q[5], goff1, rt);
#pragma unroll
      for (int t = 0; t < 4; ++t)
#pragma unroll
        for (int r = 0; r < 4; ++r) accB = __builtin_fmaf(z[t][r], W[tile_id(t, 5)][r], accB);
#pragma unroll
      for (int t = 0; t < 4; ++t) tile(accB, t, 6);
      asm volatile("" : "+v"(accB));
      wait_vm<16>();
      bar(); SCHED();
      gat16<32768>(mbW, q[6], goff0, rt); gat16<33792>(mbW, q[7], goff1, rt); gat16<34816>(mbW, q[8], goff0, rt); gat16<35840>(mbW, q[9], goff1, rt);
      gat16<0>(mbG - 32768, q[10], goff0, rt); gat16<1024>(mbG - 32768, q[11], goff1, rt);
      SCHED();
      __builtin_amdgcn_s_setprio(0);
      tile(accB, 4, 4); tile(accB, 4, 5); tile(accB, 4, 6); tile(accB, 5, 5); tile(accB, 5, 6); tile(accB, 6, 6);
      const float t13 = wave_sum63(accB);
      if (lane == 63) *(LAS float*)(size_t)lpaddr = t13;
      const unsigned fw0 = mbW + rdoff, fw1 = mbW + (rdoff ^ 64);
      auto sample = [&](auto Sc) {
        constexpr int S = decltype(Sc)::value;
        if (S == 0) wait_vm<13>(); else wait_vm<0>();
#pragma unroll
        for (int k = 0; k < 7; ++k) {
          unsigned a0, a1;
          if (k == 6) { a0 = taddr0 - 8192 - 16384 + S * 1024; a1 = taddr1 - 8192 - 16384 + S * 1024; }
          else if (k == 5) { a0 = faddr0 - 8192 - S * 32768; a1 = faddr1 - 8192 - S * 32768; }
          else if (k >= 3) { a0 = fw0 + S * 32768 + (k - 3) * 2048; a1 = fw1 + S * 32768 + (k - 3) * 2048; }
          else if (S == 0) { a0 = faddr0 - ((98 + k) & 3) * 16384; a1 = faddr1 - ((98 + k) & 3) * 16384; }
          else if (k == 2) { a0 = faddr0 - 16384; a1 = faddr1 - 16384; }
          else { a0 = fw0 + 16384 + k * 2048; a1 = fw1 + 16384 + k * 2048; }
          F[k][0] = lds128(a0); F[k][1] = lds128(a1);
        }
        float acc = 0.f;
#pragma unroll
        for (int bj = 0; bj < 7; ++bj)
#pragma unroll
          for (int bi = 0; bi <= bj; ++bi) tile(acc, bi, bj);
        const float t = wave_sum63(acc);
        if (lane == 63) *(LAS float*)(size_t)(lpaddr + 16 + S * 16) = t;
      };
      sample(IC(0));
      sample(IC(1));
    }
    bar();
  }
}

extern "C" void kernel_launch(void* const* d_in, const int* in_sizes, int n_in, void* d_out, int out_size, void* d_ws, size_t ws_size,
                              hipStream_t stream) {
  (void)in_sizes; (void)n_in; (void)out_size; (void)ws_size;
  (void)hipFuncSetAttribute((const void*)pnn_deep_kernel, hipFuncAttributeMaxDynamicSharedMemorySize, LDS_BYTES);
  pnn_deep_kernel<<<256, 512, LDS_BYTES, stream>>>((const int*)d_in[0], (const u16*)d_in[1], (const u16*)d_in[2], (const u16*)d_in[3],
                                                   (const u16*)d_in[4], (const u16*)d_in[5], (const u16*)d_in[6], (u16*)d_out, (char*)d_ws);
}
```
